```python
import torch

def _softmax(x, axis=-1):
    return torch.softmax(x, dim=axis)
def _gelu(x):
    return torch.nn.functional.gelu(x, approximate="tanh")
def _silu(x):
    return torch.nn.functional.silu(x)
def _sigmoid(x):
    return torch.sigmoid(x)
def _layernorm(x, axis=-1, eps=1e-5):
    m = x.mean(dim=axis, keepdim=True)
    v = x.var(dim=axis, keepdim=True, unbiased=False)
    return (x - m) / torch.sqrt(v + eps)
def _scatter_add(tgt, idx, src, axis=0):
    return tgt.clone().index_add_(axis, idx, src)
def _segment_sum(src, seg, n):
    out = torch.zeros((n,) + src.shape[1:], dtype=src.dtype)
    return out.index_add_(0, seg, src)
def _pad(x, pw):
    flat = tuple(v for pair in reversed(pw) for v in pair)
    return torch.nn.functional.pad(x, flat)

kernel_name = 'parallel_sum__spectral_fnet__geglu_ffn__prompt'

def setup_inputs():

    _w = (lambda a: (lambda t: t.to(torch.bfloat16) if t.is_floating_point() else t)(torch.as_tensor(a)))
    def _A_setup():
        B, S, D = 1, 2048, 512
        ns = torch.arange(S)[:, None] * torch.arange(S)[None, :]
        nd = torch.arange(D)[:, None] * torch.arange(D)[None, :]
        Ws_re = torch.cos(-2.0 * torch.pi * ns / S).to(float)
        Ws_im = torch.sin(-2.0 * torch.pi * ns / S).to(float)
        Wd_re = torch.cos(-2.0 * torch.pi * nd / D).to(float)
        Wd_im = torch.sin(-2.0 * torch.pi * nd / D).to(float)
        return {
            "x": _w((lambda *s: torch.randn(*s, dtype=torch.bfloat16) * 0.05)(B, S, D)),
            "dft_seq_re": _w(Ws_re),
            "dft_seq_im": _w(Ws_im),
            "dft_hid_re": _w(Wd_re),
            "dft_hid_im": _w(Wd_im),
        }
    _A_in = _A_setup()

    def _B_setup():
        B, S, D, F = 1, 2048, 512, 1792
        return {
            "x": _w((lambda *s: torch.randn(*s, dtype=torch.bfloat16) * 0.05)(B, S, D)),
            "w_gate": _w((lambda *s: torch.randn(*s, dtype=torch.bfloat16) * 0.2)(D, F)),
            "w_up": _w((lambda *s: torch.randn(*s, dtype=torch.bfloat16) * 0.2)(D, F)),
            "w_down": _w((lambda *s: torch.randn(*s, dtype=torch.bfloat16) * 0.2)(F, D)),
        }
    _B_in = _B_setup()

    return {
        "A_x": _A_in["x"],
        "A_dft_seq_re": _A_in["dft_seq_re"],
        "A_dft_seq_im": _A_in["dft_seq_im"],
        "A_dft_hid_re": _A_in["dft_hid_re"],
        "A_dft_hid_im": _A_in["dft_hid_im"],
        "B_x": _B_in["x"],
        "B_w_gate": _B_in["w_gate"],
        "B_w_up": _B_in["w_up"],
        "B_w_down": _B_in["w_down"],
    }

def reference(A_x, A_dft_seq_re, A_dft_seq_im, A_dft_hid_re, A_dft_hid_im, B_x, B_w_gate, B_w_up, B_w_down):
    def _A_ref(x, dft_seq_re, dft_seq_im, dft_hid_re, dft_hid_im):
        a = torch.einsum("bsd,ed->bse", x, dft_hid_re)
        b = torch.einsum("bsd,ed->bse", x, dft_hid_im)
        y = torch.einsum("ts,bse->bte", dft_seq_re, a) - torch.einsum("ts,bse->bte", dft_seq_im, b)
        return y

    def _B_ref(x, w_gate, w_up, w_down):
        return (_gelu(x @ w_gate) * (x @ w_up)) @ w_down

    x = A_x
    xn = _layernorm(x, axis=-1)
    return x + _A_ref(xn, A_dft_seq_re, A_dft_seq_im, A_dft_hid_re, A_dft_hid_im) + _B_ref(xn, B_w_gate, B_w_up, B_w_down)
```

```cpp
#include <hip/hip_runtime.h>
#include <hip/hip_bf16.h>
#include <stdint.h>

typedef unsigned short u16;
typedef __attribute__((ext_vector_type(8))) short bf16x8;
typedef __attribute__((ext_vector_type(4))) short s16x4;
typedef __attribute__((ext_vector_type(16))) float f32x16;
typedef __attribute__((ext_vector_type(4))) float f32x4;
typedef __attribute__((ext_vector_type(2))) float f32x2;
typedef __attribute__((ext_vector_type(2))) __bf16 bf2;
typedef __attribute__((ext_vector_type(4))) unsigned u32x4;
typedef __attribute__((ext_vector_type(2))) unsigned u32x2;
#define LDSP(T, p) ((__attribute__((address_space(3))) T*)(p))

#define SB() __builtin_amdgcn_sched_barrier(0)
constexpr int S = 2048, D = 512, F = 1792;
constexpr size_t WS_BP = 0;
constexpr size_t WS_H = WS_BP + (size_t)512 * 512 * 16;
constexpr size_t WS_P = WS_H + (size_t)224 * 2048 * 16;
constexpr size_t WS_FLAG = WS_P + (size_t)64 * 16 * 8192;

template <int N> __device__ __forceinline__ void wait_vm() { asm volatile("s_waitcnt vmcnt(%0)" ::"n"(N) : "memory"); }
__device__ __forceinline__ void bar() {
  asm volatile("" ::: "memory");
  __builtin_amdgcn_s_barrier();
  asm volatile("" ::: "memory");
}
__device__ __forceinline__ unsigned pack2(float a, float b) {
  f32x2 p = {a, b};
  return __builtin_bit_cast(unsigned, __builtin_convertvector(p, bf2));
}
__device__ __forceinline__ float bflo(unsigned u) { return __builtin_bit_cast(float, u << 16); }
__device__ __forceinline__ float bfhi(unsigned u) { return __builtin_bit_cast(float, u & 0xffff0000u); }
__device__ __forceinline__ float bf2f(u16 u) { return __builtin_bit_cast(float, (unsigned)u << 16); }

#define STAMP(k, i)
typedef __amdgpu_buffer_rsrc_t rsrc_t;
__device__ __forceinline__ rsrc_t mk_rsrc(const void* p, unsigned bytes) {
  return __builtin_amdgcn_make_buffer_rsrc((void*)p, 0, bytes, 0x00020000);
}
template <int AUX>
__device__ __forceinline__ void glds16(rsrc_t r, char* lds, unsigned voff, unsigned soff) {
  __builtin_amdgcn_raw_ptr_buffer_load_lds(r, LDSP(void, lds), 16, voff, soff, 0, AUX);
}
__device__ __forceinline__ void wait_lgkm0() { asm volatile("s_waitcnt lgkmcnt(0)" ::: "memory"); }
__device__ __forceinline__ bf16x8 tr_frag(const char* p0, const char* p1) {
  s16x4 a = __builtin_amdgcn_ds_read_tr16_b64_v4i16(LDSP(s16x4, p0));
  s16x4 b = __builtin_amdgcn_ds_read_tr16_b64_v4i16(LDSP(s16x4, p1));
  return __builtin_shufflevector(a, b, 0, 1, 2, 3, 4, 5, 6, 7);
}
__device__ __forceinline__ float gelu_mul(float g, float u) {
  float z = g * (-2.302208198f - 0.1029432397f * g * g);
  float e = __builtin_amdgcn_exp2f(z);
  return g * u * __builtin_amdgcn_rcpf(1.0f + e);
}
__device__ __forceinline__ float sum2(unsigned w, float acc) {
  return __builtin_amdgcn_fdot2_f32_bf16(__builtin_bit_cast(bf2, w), __builtin_bit_cast(bf2, 0x3f803f80u), acc, false);
}
__device__ __forceinline__ unsigned lds_addr(const char* p) { return (unsigned)(size_t)LDSP(const char, p); }
#define AUX_ST 16
#define MFMA16(a, b, c) __builtin_amdgcn_mfma_f32_16x16x32_bf16(a, b, c, 0, 0, 0)

struct T { static constexpr bool value = true; };
struct N { static constexpr bool value = false; };
__device__ __forceinline__ int swq(int row) { return (0x78 >> (((row >> 2) & 3) * 2)) & 3; }
constexpr int K1_R = 131072;
constexpr int K1_SLOT0 = K1_R + 8192;
constexpr int K1_STAT = K1_R;
constexpr int K1_CS = K1_R + 1024;

__global__ __launch_bounds__(512, 2) void k_s1(const u16* __restrict__ x, const u16* __restrict__ wre,
                                               const u16* __restrict__ wim, const u16* __restrict__ wg,
                                               const u16* __restrict__ wu, char* __restrict__ ws) {
  __shared__ __attribute__((aligned(1024))) char shm[163840];
  const int tid = threadIdx.x, lane = tid & 63;
  const int wid = __builtin_amdgcn_readfirstlane(tid >> 6);
  const int id = blockIdx.x, xcd = id & 7, idx = id >> 3;
  const int rb = (xcd & 1) * 8 + (idx >> 2), cg = (xcd >> 1) * 4 + (idx & 3);
  const bool isQ = (cg & 3) == 3;
  const int unit1 = 16 + (cg >> 2) * 3 + (cg & 3);
  const int unit2 = cg;
  const int r = lane & 15, g = lane >> 4;

  if (wid >= 4) {
    const int lw = wid - 4;
    if (tid == 256) ((unsigned*)(ws + WS_FLAG))[blockIdx.x * 32] = 0;
    const rsrc_t rx = mk_rsrc(x, S * D * 2);
    const rsrc_t rre = mk_rsrc(wre, D * D * 2);
    const rsrc_t rim = mk_rsrc(wim, D * D * 2);
    const rsrc_t rw = mk_rsrc((lw & 2) ? wu : wg, D * F * 2);
    unsigned vx[4];
#pragma unroll
    for (int i = 0; i < 4; ++i) {
      const int row = lw * 32 + i * 8 + (lane >> 3);
      vx[i] = (rb * 128 + row) * (D * 2) + (((lane & 7) ^ ((row >> 1) & 7)) << 4);
    }
    unsigned vd;
    {
      const int row = lw * 8 + (lane >> 3);
      vd = (cg * 32 + row) * (D * 2) + (((lane & 7) ^ ((row >> 1) & 7)) << 4);
    }
    unsigned vw[2], vq[2];
#pragma unroll
    for (int i = 0; i < 2; ++i) {
      const int row = (lw & 1) * 16 + i * 8 + (lane >> 3);
      const int f = ((row >> 1) & 1) | (((row >> 3) & 1) << 1);
      vw[i] = row * (F * 2) + (((lane & 7) ^ (f << 1)) << 4);
      const int e = lw * 32 + i * 16 + (lane >> 2);
      vq[i] = ((cg >> 2) * 128 + e) * (D * 2) + (((lane & 3) ^ swq(e)) << 4);
    }
    auto issueW = [&](int j, int slot) {
      char* dst = shm + K1_SLOT0 + slot * 8192 + lw * 2048;
      if (j < 16 && isQ) {
        glds16<0>(rim, dst, vq[0], j * 64);
        glds16<0>(rim, dst + 1024, vq[1], j * 64);
      } else {
        const unsigned so = (j & 15) * (32 * F * 2) + (j < 16 ? unit1 : unit2) * 128;
        glds16<0>(rw, dst, vw[0], so);
        glds16<0>(rw, dst + 1024, vw[1], so);
      }
    };
#pragma unroll 1
    for (int i = 0; i < 8; ++i) {
#pragma unroll
      for (int q = 0; q < 4; ++q) glds16<0>(rx, shm + i * 16384 + lw * 4096 + q * 1024, vx[q], i * 128);
      glds16<0>(rre, shm + K1_R + i * 4096 + lw * 1024, vd, i * 128);
    }
    wait_vm<35>(); bar();
    wait_vm<30>(); bar();
    wait_vm<25>(); bar();
    wait_vm<20>(); bar();
    wait_vm<15>(); bar();
    issueW(0, 0);
    wait_vm<12>(); bar();
    wait_vm<7>(); bar();
    issueW(1, 1);
    wait_vm<4>(); bar();
    wait_vm<2>(); bar();
    issueW(2, 2);
    int ca[2];
    if (true) {
      const int q = (lane & 15) >> 2, p = lane & 3;
#pragma unroll
      for (int t = 0; t < 2; ++t) {
        const int row = 8 * g + 4 * t + q;
        const int f = ((row >> 1) & 1) | (((row >> 3) & 1) << 1);
        ca[t] = (lw >> 1) * 4096 + row * 128 + ((((lw & 1) * 2) ^ f) << 5) + p * 8;
      }
    }
    const int cq = (lw * 32 + r) * 64 + ((g ^ swq(r)) << 4);
    float cs0 = 0.f, cs1 = 0.f;
    int slot = 0;
#pragma unroll 1
    for (int j = 0; j < 32; ++j) {
      const char* st = shm + K1_SLOT0 + slot * 8192;
      u32x4 f0, f1;
      const unsigned sa = lds_addr(st);
      if (j < 16 && isQ) {
        asm volatile("ds_read_b128 %0, %2\n\tds_read_b128 %1, %2 offset:1024\n\ts_waitcnt lgkmcnt(0)"
                     : "=&v"(f0), "=&v"(f1) : "v"(sa + cq) : "memory");
      } else {
        u32x2 t0, t1, t2, t3;
        asm volatile("ds_read_b64_tr_b16 %0, %4\n\tds_read_b64_tr_b16 %1, %5\n\tds_read_b64_tr_b16 %2, %6\n\t"
                     "ds_read_b64_tr_b16 %3, %7\n\ts_waitcnt lgkmcnt(0)"
                     : "=&v"(t0), "=&v"(t1), "=&v"(t2), "=&v"(t3)
                     : "v"(sa + ca[0]), "v"(sa + ca[1]), "v"(sa + (ca[0] ^ 32)), "v"(sa + (ca[1] ^ 32)) : "memory");
        f0 = u32x4{t0[0], t0[1], t1[0], t1[1]};
        f1 = u32x4{t2[0], t2[1], t3[0], t3[1]};
      }
#pragma unroll
      for (int q = 0; q < 4; ++q) { cs0 = sum2(f0[q], cs0); cs1 = sum2(f1[q], cs1); }
      if ((j & 15) == 15) {
        cs0 += __shfl_xor(cs0, 16); cs0 += __shfl_xor(cs0, 32);
        cs1 += __shfl_xor(cs1, 16); cs1 += __shfl_xor(cs1, 32);
        if (lane < 32) ((float*)(shm + K1_CS))[(j >> 4) * 128 + lw * 32 + lane] = lane < 16 ? cs0 : cs1;
        cs0 = 0.f; cs1 = 0.f;
      }
      wait_lgkm0();
      if (j < 30) wait_vm<2>(); else wait_vm<0>();
      bar();
      if (j + 3 < 32) issueW(j + 3, slot);
      slot = slot == 2 ? 0 : slot + 1;
    }
    return;
  }

  STAMP(0, 0);
  const int wr = wid >> 1, wc = wid & 1;
  const int sw = (r >> 1) & 7;
  int xa[2];
#pragma unroll
  for (int j = 0; j < 2; ++j) xa[j] = (wr * 64 + r) * 128 + (((4 * j + g) ^ sw) << 4);
  const rsrc_t rbp = mk_rsrc(ws + WS_BP, 512 * D * 16);
  const rsrc_t rh = mk_rsrc(ws + WS_H, 224 * S * 16);
  const bf16x8 ones = {0x3f80, 0x3f80, 0x3f80, 0x3f80, 0x3f80, 0x3f80, 0x3f80, 0x3f80};

  f32x4 a0[4] = {}, ssum[2] = {}, ssq[2] = {}, cs32 = {};
#pragma unroll 1
  for (int i = 0; i < 8; ++i) {
    bar();
    if (i == 0) STAMP(0, 1);
#pragma unroll
    for (int j = 0; j < 2; ++j) {
      bf16x8 xf[4];
#pragma unroll
      for (int b = 0; b < 4; ++b) xf[b] = *(const bf16x8*)(shm + i * 16384 + xa[j] + b * 2048);
      const bf16x8 wf = *(const bf16x8*)(shm + K1_R + i * 4096 + (wc * 16 + r) * 128 + (((4 * j + g) ^ sw) << 4));
#pragma unroll
      for (int b = 0; b < 4; ++b) a0[b] = MFMA16(xf[b], wf, a0[b]);
      cs32 = MFMA16(ones, wf, cs32);
      const bf16x8 s0 = wc ? xf[2] : xf[0], s1 = wc ? xf[3] : xf[1];
      ssum[0] = MFMA16(s0, ones, ssum[0]);
      ssum[1] = MFMA16(s1, ones, ssum[1]);
      ssq[0] = MFMA16(s0, s0, ssq[0]);
      ssq[1] = MFMA16(s1, s1, ssq[1]);
    }
    wait_lgkm0();
  }
  if (g == (r >> 2)) {
#pragma unroll
    for (int b = 0; b < 2; ++b) {
      const int q = r & 3;
      const float sm = q == 0 ? ssum[b][0] : q == 1 ? ssum[b][1] : q == 2 ? ssum[b][2] : ssum[b][3];
      const float sq = q == 0 ? ssq[b][0] : q == 1 ? ssq[b][1] : q == 2 ? ssq[b][2] : ssq[b][3];
      const float m = sm * (1.0f / D);
      const float var = fmaxf(sq * (1.0f / D) - m * m, 0.f);
      const float rs = __builtin_amdgcn_rsqf(var + 1e-5f);
      f32x2 st = {rs, m * rs};
      *(f32x2*)(shm + K1_STAT + (wr * 64 + wc * 32 + b * 16 + r) * 8) = st;
    }
  }
  wait_lgkm0();
  bar();
  STAMP(0, 2);

  int wa[2];
  {
    const int q = (lane & 15) >> 2, p = lane & 3;
#pragma unroll
    for (int t = 0; t < 2; ++t) {
      const int row = 8 * g + 4 * t + q;
      const int f = ((row >> 1) & 1) | (((row >> 3) & 1) << 1);
      wa[t] = row * 128 + (((wc * 2) ^ f) << 5) + p * 8;
    }
  }
  const int wq = (wc * 64 + r) * 64 + ((g ^ swq(r)) << 4);
  f32x4 acc[4][4];
  bf16x8 fx[2][4], fw[2][4];
  auto rdx = [&](int buf, int b, int j) { fx[buf][b] = *(const bf16x8*)(shm + ((j & 15) >> 1) * 16384 + ((j & 1) ? xa[1] : xa[0]) + b * 2048); };
  auto rdw = [&](int buf, int b, const char* st, bool q) {
    if (q) fw[buf][b] = *(const bf16x8*)(st + wq + b * 1024);
    else fw[buf][b] = tr_frag(st + (wa[0] ^ ((b & 1) << 5)) + (b >> 1) * 4096, st + (wa[1] ^ ((b & 1) << 5)) + (b >> 1) * 4096);
  };
  auto sub = [&](auto qc, auto qn, int mb, int jn, const char* stn, bool doread) {
    const int nb = mb ^ 1;
#pragma unroll
    for (int wi = 0; wi < 4; ++wi) {
#pragma unroll
      for (int xi = 0; xi < 4; ++xi) {
        if (decltype(qc)::value) acc[wi][xi] = MFMA16(fx[mb][xi], fw[mb][wi], acc[wi][xi]);
        else acc[wi][xi] = MFMA16(fw[mb][wi], fx[mb][xi], acc[wi][xi]);
        SB();
        if (doread) {
          const int n = wi * 4 + xi;
          if (n == 0) rdw(nb, 0, stn, decltype(qn)::value);
          if (n == 2) rdw(nb, 1, stn, decltype(qn)::value);
          if (n == 4) rdw(nb, 2, stn, decltype(qn)::value);
          if (n == 6) rdw(nb, 3, stn, decltype(qn)::value);
          if (n == 8) rdx(nb, 0, jn);
          if (n == 9) rdx(nb, 1, jn);
          if (n == 10) rdx(nb, 2, jn);
          if (n == 11) rdx(nb, 3, jn);
        }
        SB();
      }
    }
  };
  auto epi_dft = [&](const f32x4* a, int nb_e, float cs, int e, int chunk0, float sgn) {
#pragma unroll
    for (int sp = 0; sp < 2; ++sp) {
      unsigned o[2][2];
#pragma unroll
      for (int h = 0; h < 2; ++h) {
        const f32x4 v = a[(2 * sp + h) * nb_e];
        const f32x4 s01 = *(const f32x4*)(shm + K1_STAT + (wr * 64 + (2 * sp + h) * 16 + 4 * g) * 8);
        const f32x4 s23 = *(const f32x4*)(shm + K1_STAT + (wr * 64 + (2 * sp + h) * 16 + 4 * g + 2) * 8);
        o[h][0] = pack2(sgn * (v[0] * s01[0] - s01[1] * cs), sgn * (v[1] * s01[2] - s01[3] * cs));
        o[h][1] = pack2(sgn * (v[2] * s23[0] - s23[1] * cs), sgn * (v[3] * s23[2] - s23[3] * cs));
      }
      auto s0 = __builtin_amdgcn_permlane16_swap(o[0][0], o[1][0], false, false);
      auto s1 = __builtin_amdgcn_permlane16_swap(o[0][1], o[1][1], false, false);
      u32x4 v = {s0[0], s1[0], s0[1], s1[1]};
      const int chunk = chunk0 + (rb * 128 + wr * 64 + sp * 32) / 8 + (g & 1) * 2 + (g >> 1);
      __builtin_amdgcn_raw_buffer_store_b128(v, rbp, (chunk * D + e) * 16, 0, AUX_ST);
    }
  };
  auto epi_ffn = [&](int ps, int unit) {
    f32x4 cg4[2], cu4[2];
#pragma unroll
    for (int fi = 0; fi < 2; ++fi) {
      cg4[fi] = *(const f32x4*)(shm + K1_CS + (ps * 128 + wc * 32 + fi * 16 + 4 * g) * 4);
      cu4[fi] = *(const f32x4*)(shm + K1_CS + (ps * 128 + 64 + wc * 32 + fi * 16 + 4 * g) * 4);
    }
#pragma unroll
    for (int ti = 0; ti < 4; ++ti) {
      const f32x2 st = *(const f32x2*)(shm + K1_STAT + (wr * 64 + ti * 16 + r) * 8);
      unsigned o[2][2];
#pragma unroll
      for (int fi = 0; fi < 2; ++fi) {
        float h[4];
#pragma unroll
        for (int q = 0; q < 4; ++q)
          h[q] = gelu_mul(acc[fi][ti][q] * st[0] - st[1] * cg4[fi][q], acc[2 + fi][ti][q] * st[0] - st[1] * cu4[fi][q]);
        o[fi][0] = pack2(h[0], h[1]);
        o[fi][1] = pack2(h[2], h[3]);
      }
      auto s0 = __builtin_amdgcn_permlane16_swap(o[0][0], o[1][0], false, false);
      auto s1 = __builtin_amdgcn_permlane16_swap(o[0][1], o[1][1], false, false);
      u32x4 v = {s0[0], s1[0], s0[1], s1[1]};
      const int chunk = unit * 8 + wc * 4 + (g & 1) * 2 + (g >> 1);
      __builtin_amdgcn_raw_buffer_store_b128(v, rh, (chunk * S + rb * 128 + wr * 64 + ti * 16 + r) * 16, 0, AUX_ST);
    }
  };
  auto zero_acc = [&]() {
#pragma unroll
    for (int a = 0; a < 4; ++a)
#pragma unroll
      for (int b = 0; b < 4; ++b) acc[a][b] = f32x4{0.f, 0.f, 0.f, 0.f};
  };
  const char* slots = shm + K1_SLOT0;
  auto pass = [&](auto qc, int j0, int& slot, bool last) {
#pragma unroll 1
    for (int j = j0; j < j0 + 14; j += 2) {
      wait_lgkm0(); bar();
      slot = slot == 2 ? 0 : slot + 1;
      sub(qc, qc, 0, j + 1, slots + slot * 8192, true);
      wait_lgkm0(); bar();
      slot = slot == 2 ? 0 : slot + 1;
      sub(qc, qc, 1, j + 2, slots + slot * 8192, true);
    }
    wait_lgkm0(); bar();
    slot = slot == 2 ? 0 : slot + 1;
    sub(qc, qc, 0, j0 + 15, slots + slot * 8192, true);
    wait_lgkm0(); bar();
    slot = slot == 2 ? 0 : slot + 1;
    sub(qc, N{}, 1, j0 + 16, slots + slot * 8192, !last);
  };
  epi_dft(a0, 1, cs32[0], cg * 32 + wc * 16 + r, 0, 1.f);
  int slot = 0;
  zero_acc();
  if (isQ) {
#pragma unroll
    for (int b = 0; b < 4; ++b) { rdw(0, b, slots, true); rdx(0, b, 0); }
    pass(T{}, 0, slot, false);
    STAMP(0, 3);
#pragma unroll
    for (int ei = 0; ei < 4; ++ei) {
      const float cs = *(const float*)(shm + K1_CS + (wc * 64 + ei * 16 + r) * 4);
      epi_dft(&acc[ei][0], 1, cs, (cg >> 2) * 128 + wc * 64 + ei * 16 + r, 256, -1.f);
    }
  } else {
#pragma unroll
    for (int b = 0; b < 4; ++b) { rdw(0, b, slots, false); rdx(0, b, 0); }
    pass(N{}, 0, slot, false);
    STAMP(0, 3);
    epi_ffn(0, unit1);
  }
  STAMP(0, 4);
  zero_acc();
  pass(N{}, 16, slot, true);
  STAMP(0, 5);
  epi_ffn(1, unit2);
  STAMP(0, 6);
}

#define AUX_CS 0
#define K2_NS 4
constexpr int K2_STAGE = 32768, K2_NSTEP = 23;
__global__ __launch_bounds__(512, 2) void k_s2(const u16* __restrict__ x, const u16* __restrict__ cre,
                                               const u16* __restrict__ cim, const u16* __restrict__ wd, char* __restrict__ ws,
                                               u16* __restrict__ out) {
  __shared__ __attribute__((aligned(1024))) char shm[K2_NS * K2_STAGE];
  const int tid = threadIdx.x, lane = tid & 63;
  const int wid = __builtin_amdgcn_readfirstlane(tid >> 6);
  const int id = blockIdx.x, xcd = id & 7, idx = id >> 3;
  const int ks = xcd >> 1, mt = (xcd & 1) * 8 + (idx >> 2), nt = idx & 3;
  const int tile = mt * 4 + nt;

  if (wid >= 4) {
    const int lw = wid - 4;
    const rsrc_t rc = mk_rsrc((ks & 2) ? cim : cre, S * S * 2);
    const rsrc_t rh = mk_rsrc(ws + WS_H, 224 * S * 16);
    const rsrc_t rb = mk_rsrc(ws + WS_BP, 512 * D * 16);
    const rsrc_t rd = mk_rsrc(wd, F * D * 2);
    unsigned va[4];
#pragma unroll
    for (int i = 0; i < 4; ++i) {
      int row = lw * 32 + i * 8 + (lane >> 3);
      int ch = (lane & 7) ^ ((row >> 1) & 7);
      va[i] = (mt * 128 + row) * (S * 2) + ch * 16;
    }
    const unsigned vh = ((lw * 2) * S + mt * 128 + lane) * 16;
    const unsigned vb = ((lw * 2) * D + nt * 128 + lane) * 16;
    unsigned vd[4];
#pragma unroll
    for (int i = 0; i < 4; ++i) {
      const int row = lw * 16 + i * 4 + (lane >> 4);
      const int f = (row & 3) | (((row >> 3) & 1) << 2);
      vd[i] = row * (D * 2) + nt * 256 + (((lane & 15) ^ (f << 1)) << 4);
    }
    auto issue = [&](int step, int slot) {
      char* dst = shm + slot * K2_STAGE + lw * 4096;
      if (step < 16) {
        const unsigned so = ((ks & 1) * 16 + step) * 128;
#pragma unroll
        for (int i = 0; i < 4; ++i) glds16<AUX_CS>(rc, dst + i * 1024, va[i], so);
        const unsigned sb = (ks * 16 + step) * 8 * (D * 16);
#pragma unroll
        for (int i = 0; i < 4; ++i) glds16<0>(rb, dst + 16384 + i * 1024, vb, sb + (i >> 1) * (D * 16) + (i & 1) * 1024);
      } else {
        const unsigned so = (ks * 7 + step - 16) * (8 * S * 16);
#pragma unroll
        for (int i = 0; i < 4; ++i) glds16<0>(rh, dst + i * 1024, vh, so + (i >> 1) * (S * 16) + (i & 1) * 1024);
        const unsigned sb = (ks * 7 + step - 16) * 64 * (D * 2);
#pragma unroll
        for (int i = 0; i < 4; ++i) glds16<0>(rd, dst + 16384 + i * 1024, vd[i], sb);
      }
    };
#pragma unroll 1
    for (int s0 = 0; s0 < K2_NS - 1; ++s0) issue(s0, s0);
    int slot = K2_NS - 1;
#pragma unroll 1
    for (int i = 0; i < K2_NSTEP; ++i) {
      if (i + K2_NS - 2 < K2_NSTEP) wait_vm<8 * (K2_NS - 3)>(); else wait_vm<0>();
      bar();
      if (i + K2_NS - 1 < K2_NSTEP) issue(i + K2_NS - 1, slot);
      slot = slot == K2_NS - 1 ? 0 : slot + 1;
    }
    return;
  }

  STAMP(1, 0);
  const int wr = wid >> 1, wc = wid & 1;
  const int r = lane & 15, g = lane >> 4;
  const int t_out = mt * 128 + wr * 64 + (ks & 1) * 32 + r;
  const int e_out = nt * 128 + wc * 64 + (ks >> 1) * 32 + g * 4;
  u32x2 xr[2][2];
#pragma unroll
  for (int ei = 0; ei < 2; ++ei)
#pragma unroll
    for (int ti = 0; ti < 2; ++ti) xr[ei][ti] = *(const u32x2*)(x + (size_t)(t_out + ti * 16) * D + e_out + ei * 16);

  f32x4 acc[4][4] = {};
  const int sw = (r >> 1) & 7;
  int ac[2];
#pragma unroll
  for (int j = 0; j < 2; ++j) ac[j] = (wr * 64 + r) * 128 + (((4 * j + g) ^ sw) << 4);
  const int ah = g * 2048 + (wr * 64 + r) * 16;
  const int bo = 16384 + g * 2048 + (wc * 64 + r) * 16;
  bf16x8 fa[2][4], fb[2][4];
  int bd[2];
  {
    const int q = (lane & 15) >> 2, p = lane & 3;
#pragma unroll
    for (int t = 0; t < 2; ++t) {
      const int row = 8 * g + 4 * t + q;
      const int f = (row & 3) | (((row >> 3) & 1) << 2);
      bd[t] = 16384 + row * 256 + (((wc * 4) ^ f) << 5) + p * 8;
    }
  }
  auto sub = [&](auto hmode, int mb, const char* st, int j, bool doread) {
    const int rb = mb ^ 1;
    auto ra = [&](int b) {
      if (decltype(hmode)::value) fa[rb][b] = *(const bf16x8*)(st + ah + j * 8192 + b * 256);
      else fa[rb][b] = *(const bf16x8*)(st + ac[j] + b * 2048);
    };
    auto rbf = [&](int b) {
      if (decltype(hmode)::value) fb[rb][b] = tr_frag(st + (bd[0] ^ (b << 5)) + j * 8192, st + (bd[1] ^ (b << 5)) + j * 8192);
      else fb[rb][b] = *(const bf16x8*)(st + bo + j * 8192 + b * 256);
    };
#pragma unroll
    for (int ei = 0; ei < 4; ++ei) {
#pragma unroll
      for (int ti = 0; ti < 4; ++ti) {
        acc[ei][ti] = MFMA16(fb[mb][ei], fa[mb][ti], acc[ei][ti]);
        SB();
        if (doread) {
          const int n = ei * 4 + ti;
          if (n == 0) ra(0);
          if (n == 2) rbf(0);
          if (n == 4) ra(1);
          if (n == 6) ra(2);
          if (n == 8) ra(3);
          if (n == 10) rbf(1);
          if (n == 12) rbf(2);
          if (n == 14) rbf(3);
        }
        SB();
      }
    }
  };
  bar();
  STAMP(1, 1);
#pragma unroll
  for (int i = 0; i < 4; ++i) {
    fa[0][i] = *(const bf16x8*)(shm + ac[0] + i * 2048);
    fb[0][i] = *(const bf16x8*)(shm + bo + i * 256);
  }
  int slot = 0;
#pragma unroll 1
  for (int i = 0; i < 15; ++i) {
    const char* cur = shm + slot * K2_STAGE;
    slot = slot == K2_NS - 1 ? 0 : slot + 1;
    const char* nxt = shm + slot * K2_STAGE;
    sub(N{}, 0, cur, 1, true);
    sub(N{}, 1, nxt, 0, true);
    bar();
    if (i == 6) STAMP(1, 6);
    if (i == 13) STAMP(1, 7);
  }
  {
    const char* cur = shm + slot * K2_STAGE;
    slot = slot == K2_NS - 1 ? 0 : slot + 1;
    const char* nxt = shm + slot * K2_STAGE;
    sub(N{}, 0, cur, 1, true);
    sub(T{}, 1, nxt, 0, true);
    bar();
  }
#pragma unroll 1
  for (int i = 16; i < K2_NSTEP - 1; ++i) {
    const char* cur = shm + slot * K2_STAGE;
    slot = slot == K2_NS - 1 ? 0 : slot + 1;
    const char* nxt = shm + slot * K2_STAGE;
    sub(T{}, 0, cur, 1, true);
    sub(T{}, 1, nxt, 0, true);
    bar();
    if (i == 20) STAMP(1, 8);
  }
  {
    const char* cur = shm + slot * K2_STAGE;
    sub(T{}, 0, cur, 1, true);
    sub(T{}, 1, cur, 0, false);
  }

  STAMP(1, 2);
  unsigned* flags = (unsigned*)(ws + WS_FLAG);
  const rsrc_t prs = mk_rsrc(ws + WS_P + (size_t)tile * 16 * 8192, 16 * 8192);
  f32x4 own[2][2];
#pragma unroll
  for (int q = 0; q < 4; ++q) {
    const int e2 = (q >> 1) * 2, t2 = (q & 1) * 2;
    if (q == ks) {
#pragma unroll
      for (int ei = 0; ei < 2; ++ei)
#pragma unroll
        for (int ti = 0; ti < 2; ++ti) own[ei][ti] = acc[e2 + ei][t2 + ti];
    } else {
      const int off = (q * 4 + ks) * 8192 + wid * 2048 + lane * 16;
#pragma unroll
      for (int ei = 0; ei < 2; ++ei) {
        const f32x4 a = acc[e2 + ei][t2], b = acc[e2 + ei][t2 + 1];
        u32x4 v = {pack2(a[0], a[1]), pack2(a[2], a[3]), pack2(b[0], b[1]), pack2(b[2], b[3])};
        __builtin_amdgcn_raw_buffer_store_b128(v, prs, off + ei * 1024, 0, 16);
      }
    }
  }
  wait_vm<0>();
  bar();
  STAMP(1, 3);
  if (wid == 0) {
    if (lane == 0) __hip_atomic_store(flags + (tile * 4 + ks) * 32, 1u, __ATOMIC_RELAXED, __HIP_MEMORY_SCOPE_AGENT);
    const int src = lane & 3;
    for (unsigned spin = 0; spin < (1u << 22); ++spin) {
      unsigned v = (src == ks) ? 1u : __hip_atomic_load(flags + (tile * 4 + src) * 32, __ATOMIC_RELAXED, __HIP_MEMORY_SCOPE_AGENT);
      if (__all(v == 1u)) break;
      __builtin_amdgcn_s_sleep(1);
    }
  }
  bar();
  STAMP(1, 4);
  u32x4 pv[3][2];
#pragma unroll
  for (int i = 0; i < 3; ++i) {
    const int src = (ks + 1 + i) & 3;
    const int off = (ks * 4 + src) * 8192 + wid * 2048 + lane * 16;
    pv[i][0] = __builtin_amdgcn_raw_buffer_load_b128(prs, off, 0, 16);
    pv[i][1] = __builtin_amdgcn_raw_buffer_load_b128(prs, off + 1024, 0, 16);
  }
#pragma unroll
  for (int i = 0; i < 3; ++i)
#pragma unroll
    for (int ei = 0; ei < 2; ++ei)
#pragma unroll
      for (int ti = 0; ti < 2; ++ti) {
        own[ei][ti][0] += bflo(pv[i][ei][2 * ti]);
        own[ei][ti][1] += bfhi(pv[i][ei][2 * ti]);
        own[ei][ti][2] += bflo(pv[i][ei][2 * ti + 1]);
        own[ei][ti][3] += bfhi(pv[i][ei][2 * ti + 1]);
      }
#pragma unroll
  for (int ti = 0; ti < 2; ++ti) {
    unsigned o[2][2];
#pragma unroll
    for (int ei = 0; ei < 2; ++ei) {
      o[ei][0] = pack2(own[ei][ti][0] + bflo(xr[ei][ti][0]), own[ei][ti][1] + bfhi(xr[ei][ti][0]));
      o[ei][1] = pack2(own[ei][ti][2] + bflo(xr[ei][ti][1]), own[ei][ti][3] + bfhi(xr[ei][ti][1]));
    }
    auto s0 = __builtin_amdgcn_permlane16_swap(o[0][0], o[1][0], false, false);
    auto s1 = __builtin_amdgcn_permlane16_swap(o[0][1], o[1][1], false, false);
    u32x4 v = {s0[0], s1[0], s0[1], s1[1]};
    const int e0 = nt * 128 + wc * 64 + (ks >> 1) * 32 + (g & 1) * 16 + (g >> 1) * 8;
    *(u32x4*)(out + (size_t)(t_out + ti * 16) * D + e0) = v;
  }
  STAMP(1, 5);
}

extern "C" void kernel_launch(void* const* d_in, const int* in_sizes, int n_in, void* d_out, int out_size,
                              void* d_ws, size_t ws_size, hipStream_t stream) {
  const u16* x = (const u16*)d_in[0];
  const u16* cre = (const u16*)d_in[1];
  const u16* cim = (const u16*)d_in[2];
  const u16* wre = (const u16*)d_in[3];
  const u16* wim = (const u16*)d_in[4];
  const u16* wg = (const u16*)d_in[6];
  const u16* wu = (const u16*)d_in[7];
  const u16* wd = (const u16*)d_in[8];
  char* ws = (char*)d_ws;
  k_s1<<<256, 512, 0, stream>>>(x, wre, wim, wg, wu, ws);
  k_s2<<<256, 512, 0, stream>>>(x, cre, cim, wd, ws, (u16*)d_out);
}
```

```cpp
#include <hip/hip_runtime.h>
#include <hip/hip_bf16.h>
#include <stdint.h>

typedef unsigned short u16;
typedef unsigned long long u64;
typedef __attribute__((ext_vector_type(8))) short bf16x8;
typedef __attribute__((ext_vector_type(4))) short s16x4;
typedef __attribute__((ext_vector_type(4))) float f32x4;
typedef __attribute__((ext_vector_type(2))) float f32x2;
typedef __attribute__((ext_vector_type(2))) __bf16 bf2;
typedef __attribute__((ext_vector_type(4))) unsigned u32x4;
typedef __attribute__((ext_vector_type(2))) unsigned u32x2;
typedef __amdgpu_buffer_rsrc_t rsrc_t;
#define LDSP(T, p) ((__attribute__((address_space(3))) T*)(p))
#define SB() __builtin_amdgcn_sched_barrier(0)
#define DI __device__ __forceinline__
#define MFMA16(a, b, c) __builtin_amdgcn_mfma_f32_16x16x32_bf16(a, b, c, 0, 0, 0)

constexpr int S = 2048, D = 512, F = 1792;
constexpr size_t WS_AB = 0;
constexpr size_t WS_H = WS_AB + (size_t)4096 * 512 * 2;
constexpr size_t WS_P = WS_H + (size_t)28 * 2048 * 128;
constexpr size_t WS_FLAG = WS_P + (size_t)64 * 16 * 8192;
constexpr int FLAG_A = 0, FLAG_B = 256 * 128, FLAG_X = 512 * 128, FLAG_E = 768 * 128;
constexpr int SLOT = 36864;
constexpr int L_STAT = 4 * SLOT;
constexpr int L_CS1 = L_STAT + 1024;
constexpr int L_CS2 = L_STAT + 3072;
constexpr int L_DUMMY = L_STAT + 5120;
constexpr int L_POLL = L_STAT + 6144;
constexpr int LDS_BYTES = L_STAT + 8192;

template <int N> DI void wait_vm() { asm volatile("s_waitcnt vmcnt(%0)" ::"n"(N) : "memory"); }
DI void wait_lgkm0() { asm volatile("s_waitcnt lgkmcnt(0)" ::: "memory"); }
DI void bar() {
  asm volatile("" ::: "memory");
  __builtin_amdgcn_s_barrier();
  asm volatile("" ::: "memory");
}
DI unsigned pack2(float a, float b) {
  f32x2 p = {a, b};
  return __builtin_bit_cast(unsigned, __builtin_convertvector(p, bf2));
}
DI float bflo(unsigned u) { return __builtin_bit_cast(float, u << 16); }
DI float bfhi(unsigned u) { return __builtin_bit_cast(float, u & 0xffff0000u); }
DI rsrc_t mk_rsrc(const void* p, unsigned bytes) { return __builtin_amdgcn_make_buffer_rsrc((void*)p, 0, bytes, 0x00020000); }
DI void dma16(rsrc_t r, unsigned lds, unsigned voff, unsigned soff) {
  asm volatile("s_mov_b32 m0, %0\n\ts_nop 0\n\tbuffer_load_dwordx4 %1, %2, %3 offen lds" ::"s"(lds), "v"(voff), "s"(r), "s"(soff) : "memory");
}
DI void dma16c(rsrc_t r, unsigned lds, unsigned voff, unsigned soff) {
  asm volatile("s_mov_b32 m0, %0\n\ts_nop 0\n\tbuffer_load_dwordx4 %1, %2, %3 offen sc1 lds" ::"s"(lds), "v"(voff), "s"(r), "s"(soff) : "memory");
}
DI void dma4(rsrc_t r, unsigned lds, unsigned voff, unsigned soff) {
  asm volatile("s_mov_b32 m0, %0\n\ts_nop 0\n\tbuffer_load_dword %1, %2, %3 offen lds" ::"s"(lds), "v"(voff), "s"(r), "s"(soff) : "memory");
}
DI void dma16_lo32(rsrc_t r, unsigned lds, unsigned voff, unsigned soff) {
  asm volatile("s_mov_b32 m0, %0\n\ts_mov_b32 exec_hi, 0\n\tbuffer_load_dwordx4 %1, %2, %3 offen lds\n\ts_mov_b32 exec_hi, -1"
               ::"s"(lds), "v"(voff), "s"(r), "s"(soff) : "memory");
}
DI void dma4_lo8(rsrc_t r, unsigned lds, unsigned voff, unsigned soff) {
  asm volatile("s_mov_b32 m0, %0\n\ts_mov_b64 exec, 0xff\n\tbuffer_load_dword %1, %2, %3 offen lds\n\ts_mov_b64 exec, -1"
               ::"s"(lds), "v"(voff), "s"(r), "s"(soff) : "memory");
}
DI unsigned opq(unsigned v) { asm volatile("" : "+v"(v)); return v; }
DI bf16x8 rd128(unsigned a) { return *(const __attribute__((address_space(3))) bf16x8*)(size_t)a; }
DI bf16x8 rdtr(unsigned a0, unsigned a1) {
  s16x4 a = __builtin_amdgcn_ds_read_tr16_b64_v4i16((__attribute__((address_space(3))) s16x4*)(size_t)a0);
  s16x4 b = __builtin_amdgcn_ds_read_tr16_b64_v4i16((__attribute__((address_space(3))) s16x4*)(size_t)a1);
  return __builtin_shufflevector(a, b, 0, 1, 2, 3, 4, 5, 6, 7);
}
DI bf16x8 tr_frag(const char* p0, const char* p1) {
  s16x4 a = __builtin_amdgcn_ds_read_tr16_b64_v4i16(LDSP(s16x4, p0));
  s16x4 b = __builtin_amdgcn_ds_read_tr16_b64_v4i16(LDSP(s16x4, p1));
  return __builtin_shufflevector(a, b, 0, 1, 2, 3, 4, 5, 6, 7);
}
DI float gelu_mul(float g, float u) {
  float z = g * (-2.302208198f - 0.1029432397f * g * g);
  float e = __builtin_amdgcn_exp2f(z);
  return g * u * __builtin_amdgcn_rcpf(1.0f + e);
}
DI float dot2(unsigned a, unsigned b, float acc) {
  return __builtin_amdgcn_fdot2_f32_bf16(__builtin_bit_cast(bf2, a), __builtin_bit_cast(bf2, b), acc, false);
}
DI float frag_sum(bf16x8 f, float acc) {
  const u32x4 u = __builtin_bit_cast(u32x4, f);
#pragma unroll
  for (int i = 0; i < 4; ++i) { const unsigned w = u[i]; acc = dot2(w, 0x3f803f80u, acc); }
  return acc;
}
DI float frag_sq(bf16x8 f, float acc) {
  const u32x4 u = __builtin_bit_cast(u32x4, f);
#pragma unroll
  for (int i = 0; i < 4; ++i) { const unsigned w = u[i]; acc = dot2(w, w, acc); }
  return acc;
}
DI float red_g(float v) {
  v += __shfl_xor(v, 16);
  v += __shfl_xor(v, 32);
  return v;
}
DI u32x4 swap16(unsigned a0, unsigned a1, unsigned b0, unsigned b1) {
  auto s0 = __builtin_amdgcn_permlane16_swap(a0, b0, false, false);
  auto s1 = __builtin_amdgcn_permlane16_swap(a1, b1, false, false);
  return u32x4{s0[0], s1[0], s0[1], s1[1]};
}
DI u64 ld_flag(const char* p) { return __hip_atomic_load((const u64*)p, __ATOMIC_RELAXED, __HIP_MEMORY_SCOPE_AGENT); }
DI void st_flag(char* p, u64 v) { __hip_atomic_store((u64*)p, v, __ATOMIC_RELAXED, __HIP_MEMORY_SCOPE_AGENT); }
DI void spin_flags(const char* p, u64 tag) {
#pragma unroll 1
  for (unsigned spin = 0; spin < (1u << 20); ++spin) {
    if (__all(ld_flag(p) == tag)) break;
    __builtin_amdgcn_s_sleep(1);
  }
}

#define STAMP(k, i)

constexpr int st_ops(int stage) { return stage < 8 ? 5 : (stage < 12 ? 4 : 0); }
constexpr int pf_ops(int n) { return (n >= 0 && n < 8) ? 1 : 0; }
constexpr int allow(int n) { return pf_ops(n - 3) + st_ops(n + 1) + pf_ops(n - 2) + st_ops(n + 2) + pf_ops(n - 1); }

__global__ __launch_bounds__(512, 2) void k_fused(const u16* __restrict__ x, const u16* __restrict__ cre,
                                                  const u16* __restrict__ cim, const u16* __restrict__ wre,
                                                  const u16* __restrict__ wim, const u16* __restrict__ wg,
                                                  const u16* __restrict__ wu, const u16* __restrict__ wd,
                                                  char* __restrict__ ws, u16* __restrict__ out) {
  __shared__ __attribute__((aligned(1024))) char shm[LDS_BYTES];
  const unsigned lds0 = (unsigned)(size_t)LDSP(char, shm);
  const int tid = threadIdx.x, lane = tid & 63;
  const int wid = __builtin_amdgcn_readfirstlane(tid >> 6);
  const int id = blockIdx.x, xcd = id & 7, idx = id >> 3;
  const int c = xcd >> 1, cgl = idx & 3;
  const int rb = (xcd & 1) * 8 + (idx >> 2);
  const bool isQ = cgl == 3;
  const int unit1 = 7 * c + 4 + cgl, unit2 = 7 * c + cgl;
  const int rot = ((idx >> 2) + 2 * cgl) & 7;
  const int r = lane & 15, g = lane >> 4;
  const int wr = wid & 3, gq = wid >> 2;
  char* flags = ws + WS_FLAG;
  STAMP(0, 0);

  const rsrc_t rx = mk_rsrc(x, S * D * 2);
  const rsrc_t rre = mk_rsrc(wre, D * D * 2);
  const rsrc_t rw = mk_rsrc(gq ? wu : wg, D * F * 2);
  const rsrc_t r1 = isQ ? mk_rsrc(wim, D * D * 2) : rw;
  const rsrc_t rc = mk_rsrc((c & 2) ? cim : cre, S * S * 2);
  unsigned vrow[2], vw[2], v1[2];
#pragma unroll
  for (int i = 0; i < 2; ++i) {
    const int row = wid * 16 + i * 8 + (lane >> 3);
    vrow[i] = row * 1024 + (((lane & 7) ^ ((row >> 1) & 7)) << 4);
    const int kr = wr * 16 + i * 8 + (lane >> 3);
    const int f = ((kr >> 1) & 1) | (((kr >> 3) & 1) << 1);
    vw[i] = kr * (F * 2) + (((lane & 7) ^ (f << 1)) << 4);
    v1[i] = isQ ? vrow[i] : vw[i];
  }
  const unsigned vd32 = (wid * 4 + (lane >> 3)) * 1024 + (((lane & 7) ^ (((wid * 4 + (lane >> 3)) >> 1) & 7)) << 4);
  const unsigned base1 = isQ ? c * 131072 : unit1 * 128, step1 = isQ ? 128 : 64 * F * 2;
  auto issueA = [&](int stage) {
    const unsigned sl = lds0 + (stage & 3) * SLOT + wid * 2048;
    if (stage < 8) {
      const int kp = (stage + rot) & 7;
      dma16(rx, sl, vrow[0], rb * 131072 + kp * 128);
      dma16(rx, sl + 1024, vrow[1], rb * 131072 + kp * 128);
      dma16(r1, sl + 16384, v1[0], base1 + kp * step1);
      dma16(r1, sl + 16384 + 1024, v1[1], base1 + kp * step1);
      dma16_lo32(rre, lds0 + (stage & 3) * SLOT + 32768 + wid * 512, vd32, (c * 4 + cgl) * 32768 + kp * 128);
    } else {
#pragma unroll
      for (int h = 0; h < 2; ++h) {
        const int kp = ((stage - 8) * 2 + h + rot) & 7;
        dma16(rw, sl + h * 16384, vw[0], unit2 * 128 + kp * (64 * F * 2));
        dma16(rw, sl + h * 16384 + 1024, vw[1], unit2 * 128 + kp * (64 * F * 2));
      }
    }
  };
  issueA(0);
  issueA(1);
  issueA(2);
  u64 tag = *(const __attribute__((address_space(4))) u64*)(flags + FLAG_E) + 1;
  asm volatile("" : "+s"(tag));
  const rsrc_t rfl = mk_rsrc(flags, FLAG_E + 128);
  auto prefetchC = [&](int n) {
    dma4_lo8(rc, lds0 + L_DUMMY, lane * 128, (rb * 128 + cgl * 32 + wid * 4 + (n >> 1)) * (S * 2) + (c & 1) * 2048 + (n & 1) * 1024);
  };

  const int sw = (r >> 1) & 7;
  const int jl = wr >> 1, bl = wr & 1;
  const unsigned xa = lds0 + r * 128 + (((4 * jl + g) ^ sw) << 4);
  unsigned wa;
  {
    const int q = (lane & 15) >> 2, p = lane & 3;
    const int f = (q >> 1) | ((g & 1) << 1);
    wa = lds0 + (8 * g + q) * 128 + (((2 * gq + bl) ^ f) << 5) + p * 8 + jl * 4096;
  }
  bf16x8 xf[8][2][2];
  bf16x8 wf[4][2], df[2][2];
  f32x4 acc[4][2], a0[2];
  float ssum = 0.f, ssq = 0.f, cs0 = 0.f, cs1 = 0.f, csd = 0.f;
#pragma unroll
  for (int a = 0; a < 4; ++a) { acc[a][0] = f32x4{0.f, 0.f, 0.f, 0.f}; acc[a][1] = f32x4{0.f, 0.f, 0.f, 0.f}; }
  a0[0] = f32x4{0.f, 0.f, 0.f, 0.f}; a0[1] = f32x4{0.f, 0.f, 0.f, 0.f};

  auto read_w_tr = [&](unsigned st) {
    const unsigned w0 = opq(wa);
#pragma unroll
    for (int j = 0; j < 2; ++j)
#pragma unroll
      for (int f1 = 0; f1 < 2; ++f1) {
        const unsigned o = (w0 ^ (f1 << 5) ^ (j << 12)) + st;
        wf[f1][j] = rdtr(o, o + 512);
        wf[2 + f1][j] = rdtr(o + 8192, o + 8192 + 512);
      }
  };
  auto ffn_epi = [&](int csoff, int unit) {
    const rsrc_t rh = mk_rsrc(ws + WS_H, 28 * S * 128);
    f32x4 cg4[2], cu4[2];
#pragma unroll
    for (int fi = 0; fi < 2; ++fi) {
      const int col = gq * 32 + (fi ^ bl) * 16 + 4 * g;
      cg4[fi] = *(const f32x4*)(shm + csoff + col * 4) + *(const f32x4*)(shm + csoff + 640 + col * 4);
      cu4[fi] = *(const f32x4*)(shm + csoff + (64 + col) * 4) + *(const f32x4*)(shm + csoff + 640 + (64 + col) * 4);
    }
#pragma unroll
    for (int b = 0; b < 2; ++b) {
      const int row = wr * 32 + (b ^ gq) * 16 + r;
      const f32x2 st = *(const f32x2*)(shm + L_STAT + row * 8);
      unsigned o[2][2];
#pragma unroll
      for (int fi = 0; fi < 2; ++fi) {
        float h[4];
#pragma unroll
        for (int q = 0; q < 4; ++q)
          h[q] = gelu_mul(acc[fi][b][q] * st[0] - st[1] * cg4[fi][q], acc[2 + fi][b][q] * st[0] - st[1] * cu4[fi][q]);
        o[fi][0] = pack2(h[0], h[1]);
        o[fi][1] = pack2(h[2], h[3]);
      }
      const u32x4 v = swap16(o[0][0], o[0][1], o[1][0], o[1][1]);
      const int chunk = gq * 4 + ((g & 1) ^ bl) * 2 + (g >> 1);
      __builtin_amdgcn_raw_buffer_store_b128(v, rh, (unit * S + rb * 128 + row) * 128 + chunk * 16, 0, 16);
    }
  };
  auto dft_epi = [&](f32x4 v0, f32x4 v1_, int csidx, int row, int grow, int e0, float sgn) {
    const rsrc_t rab = mk_rsrc(ws + WS_AB, 4096 * D * 2);
    f32x2 st = *(const f32x2*)(shm + L_STAT + row * 8);
    st *= sgn;
    const int i0 = csidx + bl * 16 + 4 * g, i1 = csidx + (bl ^ 1) * 16 + 4 * g;
    const f32x4 c0 = *(const f32x4*)(shm + L_CS1 + i0 * 4) + *(const f32x4*)(shm + L_CS1 + 640 + i0 * 4);
    const f32x4 c1 = *(const f32x4*)(shm + L_CS1 + i1 * 4) + *(const f32x4*)(shm + L_CS1 + 640 + i1 * 4);
    const f32x4 y0 = v0 * st[0] - c0 * st[1], y1 = v1_ * st[0] - c1 * st[1];
    const u32x4 v = swap16(pack2(y0[0], y0[1]), pack2(y0[2], y0[3]), pack2(y1[0], y1[1]), pack2(y1[2], y1[3]));
    const int chunk = ((g & 1) ^ bl) * 2 + (g >> 1);
    __builtin_amdgcn_raw_buffer_store_b128(v, rab, (grow * D + e0) * 2 + chunk * 16, 0, 16);
  };

#pragma unroll
  for (int p = 0; p < 8; ++p) {
    if (p == 0) { wait_vm<allow(0)>(); bar(); STAMP(0, 1); }
    else if (gq == 0) {
      switch (p) {
        case 1: wait_vm<allow(1)>(); break; case 2: wait_vm<allow(2)>(); break; case 3: wait_vm<allow(3)>(); break;
        case 4: wait_vm<allow(4)>(); break; case 5: wait_vm<allow(5)>(); break; case 6: wait_vm<allow(6)>(); break;
        default: wait_vm<allow(7)>(); break;
      }
      bar();
    }
    const unsigned st = (p & 3) * SLOT;
    {
      const unsigned x0 = opq(xa);
#pragma unroll
      for (int j = 0; j < 2; ++j) {
        const unsigned xj = x0 ^ (j << 6);
#pragma unroll
        for (int b = 0; b < 2; ++b) xf[p][b][j] = rd128(xj + st + (wr * 32 + (b ^ gq) * 16) * 128);
#pragma unroll
        for (int eb = 0; eb < 2; ++eb) df[eb][j] = rd128(xj + st + 32768 + (eb ^ bl) * 2048);
      }
      if (isQ) {
#pragma unroll
        for (int j = 0; j < 2; ++j)
#pragma unroll
          for (int f1 = 0; f1 < 2; ++f1) {
            const unsigned o = (x0 ^ (j << 6)) + st + 16384 + gq * 8192 + (f1 ^ bl) * 2048;
            wf[f1][j] = rd128(o);
            wf[2 + f1][j] = rd128(o + 4096);
          }
      } else {
        read_w_tr(st + 16384);
      }
    }
    issueA(p + 3);
    prefetchC(p);
    auto sums = [&]() {
      ssum = frag_sum(xf[p][0][0], ssum); ssq = frag_sq(xf[p][0][0], ssq);
      ssum = frag_sum(xf[p][0][1], ssum); ssq = frag_sq(xf[p][0][1], ssq);
      cs0 = frag_sum(wf[0][0], cs0); cs1 = frag_sum(wf[2][0], cs1); csd = frag_sum(df[0][0], csd);
      asm volatile("" : "+v"(ssum), "+v"(ssq), "+v"(cs0), "+v"(cs1), "+v"(csd));
    };
    if (p == 7) {
      sums();
      ssum = red_g(ssum); ssq = red_g(ssq); cs0 = red_g(cs0); cs1 = red_g(cs1); csd = red_g(csd);
      const float m = ssum * (1.0f / D);
      const float var = fmaxf(ssq * (1.0f / D) - m * m, 0.f);
      const float rs = __builtin_amdgcn_rsqf(var + 1e-5f);
      if (g == 0) *(f32x2*)(shm + L_STAT + (wr * 32 + gq * 16 + r) * 8) = f32x2{rs, m * rs};
      float* cs = (float*)(shm + L_CS1 + jl * 640);
      if (g == 1) cs[(isQ ? gq * 64 : gq * 32) + bl * 16 + r] = cs0;
      if (g == 2) cs[(isQ ? gq * 64 + 32 : 64 + gq * 32) + bl * 16 + r] = cs1;
      if (g == 3) cs[128 + bl * 16 + r] = csd;
      cs0 = 0.f; cs1 = 0.f;
    }
    SB();
    if (gq == 1) {
      wait_lgkm0();
      switch (p) {
        case 0: wait_vm<allow(1)>(); break; case 1: wait_vm<allow(2)>(); break; case 2: wait_vm<allow(3)>(); break;
        case 3: wait_vm<allow(4)>(); break; case 4: wait_vm<allow(5)>(); break; case 5: wait_vm<allow(6)>(); break;
        case 6: wait_vm<allow(7)>(); break; default: wait_vm<allow(8)>(); break;
      }
      bar();
    }
    SB();
#pragma unroll
    for (int j = 0; j < 2; ++j) {
#pragma unroll
      for (int fi = 0; fi < 4; ++fi)
#pragma unroll
        for (int b = 0; b < 2; ++b) acc[fi][b] = MFMA16(wf[fi][j], xf[p][b][j], acc[fi][b]);
#pragma unroll
      for (int eb = 0; eb < 2; ++eb) a0[eb] = MFMA16(df[eb][j], xf[p][0][j], a0[eb]);
    }
    if (p < 7) sums();
    SB();
  }
  if (gq == 0) { wait_lgkm0(); wait_vm<allow(8)>(); bar(); }
  STAMP(0, 2);
  dft_epi(a0[0], a0[1], 128, wr * 32 + gq * 16 + r, rb * 128 + wr * 32 + gq * 16 + r, (c * 4 + cgl) * 32, 1.f);
  if (isQ) {
#pragma unroll
    for (int b = 0; b < 2; ++b)
#pragma unroll
      for (int h = 0; h < 2; ++h) {
        const int row = wr * 32 + (b ^ gq) * 16 + r;
        dft_epi(acc[2 * h][b], acc[2 * h + 1][b], gq * 64 + h * 32, row, S + rb * 128 + row, c * 128 + gq * 64 + h * 32, -1.f);
      }
  } else {
    ffn_epi(L_CS1, unit1);
  }
#pragma unroll
  for (int a = 0; a < 4; ++a) { acc[a][0] = f32x4{0.f, 0.f, 0.f, 0.f}; acc[a][1] = f32x4{0.f, 0.f, 0.f, 0.f}; }
  STAMP(0, 3);

  const rsrc_t rab = mk_rsrc(ws + WS_AB, 4096 * D * 2);
  unsigned vb2[2], vc2[2];
#pragma unroll
  for (int i = 0; i < 2; ++i) {
    const int row = wid * 16 + i * 8 + (lane >> 3);
    vc2[i] = row * (S * 2) + (((lane & 7) ^ ((row >> 1) & 7)) << 4);
    const int kr = wid * 8 + i * 4 + (lane >> 4);
    const int f = (kr & 3) | (((kr >> 3) & 1) << 2);
    vb2[i] = kr * 1024 + (((lane & 15) ^ (f << 1)) << 4);
  }
  auto issueB0 = [&](int step) {
    const unsigned sl = lds0 + step * SLOT + wid * 2048;
    const unsigned sc = rb * 128 * (S * 2) + ((c & 1) * 16 + step) * 128;
    dma16(rc, sl, vc2[0], sc);
    dma16(rc, sl + 1024, vc2[1], sc);
    const unsigned sb = (c * 1024 + step * 64) * 1024 + cgl * 256;
    dma16c(rab, sl + 16384, vb2[0], sb);
    dma16c(rab, sl + 16384 + 1024, vb2[1], sb);
  };
#pragma unroll
  for (int p = 0; p < 8; ++p) {
    const int n = 8 + (p >> 1);
    if ((p & 1) == 0 && p > 0 && gq == 0) {
      wait_vm<0>();
      if (p == 6 && wid == 0 && !__all(*(const u64*)(shm + L_POLL + lane * 16) == tag)) spin_flags(flags + FLAG_A + ((2 * cgl + (c & 1)) + 8 * (lane & 31)) * 128, tag);
      bar();
      if (p == 2 && tid == 0) st_flag(flags + FLAG_A + id * 128, tag);
    }
    read_w_tr((n & 3) * SLOT + (p & 1) * 16384);
    if (p == 0) issueA(11);
    if (p == 4 && wid == 0) dma16c(rfl, lds0 + L_POLL, (lane & 31) * 1024, FLAG_A + (2 * cgl + (c & 1)) * 128);
    if (p >= 6) issueB0(p - 6);
    if (p == 7) {
      cs0 = frag_sum(wf[0][0], cs0); cs1 = frag_sum(wf[2][0], cs1);
      cs0 = red_g(cs0); cs1 = red_g(cs1);
      float* cs = (float*)(shm + L_CS2 + jl * 640);
      if (g == 1) cs[gq * 32 + bl * 16 + r] = cs0;
      if (g == 2) cs[64 + gq * 32 + bl * 16 + r] = cs1;
    }
    SB();
    if ((p & 1) == 1 && gq == 1) {
      wait_lgkm0();
      if (p < 7) wait_vm<0>();
      bar();
    }
    SB();
#pragma unroll
    for (int j = 0; j < 2; ++j)
#pragma unroll
      for (int fi = 0; fi < 4; ++fi)
#pragma unroll
        for (int b = 0; b < 2; ++b) acc[fi][b] = MFMA16(wf[fi][j], xf[p][b][j], acc[fi][b]);
    if (p < 7) { cs0 = frag_sum(wf[0][0], cs0); cs1 = frag_sum(wf[2][0], cs1); asm volatile("" : "+v"(cs0), "+v"(cs1)); }
    SB();
  }
  if (gq == 0) { wait_lgkm0(); bar(); }
  STAMP(0, 4);
  issueB0(2);
  ffn_epi(L_CS2, unit2);
  STAMP(0, 5);

  const int ks = c, mt = rb, nt = cgl;
  if (gq == 1) {
    const int lw = wr;
    const rsrc_t rh = mk_rsrc(ws + WS_H, 28 * S * 128);
    const rsrc_t rd = mk_rsrc(wd, F * D * 2);
    unsigned va[4], vh[4], vb[4];
#pragma unroll
    for (int i = 0; i < 4; ++i) {
      const int row = lw * 32 + i * 8 + (lane >> 3);
      const int ch = ((lane & 7) ^ ((row >> 1) & 7)) << 4;
      va[i] = row * (S * 2) + ch;
      vh[i] = row * 128 + ch;
      const int kr = lw * 16 + i * 4 + (lane >> 4);
      const int f = (kr & 3) | (((kr >> 3) & 1) << 2);
      vb[i] = kr * 1024 + (((lane & 15) ^ (f << 1)) << 4);
    }
    auto issue = [&](int step) {
      const unsigned dst = lds0 + (step & 3) * SLOT + lw * 4096;
      if (step < 16) {
        const unsigned so = mt * 128 * (S * 2) + ((ks & 1) * 16 + step) * 128;
#pragma unroll
        for (int i = 0; i < 4; ++i) dma16(rc, dst + i * 1024, va[i], so);
        const unsigned sb = (ks * 1024 + step * 64) * 1024 + nt * 256;
#pragma unroll
        for (int i = 0; i < 4; ++i) dma16c(rab, dst + 16384 + i * 1024, vb[i], sb);
      } else {
        const unsigned so = ((ks * 7 + step - 16) * S + mt * 128) * 128;
#pragma unroll
        for (int i = 0; i < 4; ++i) dma16c(rh, dst + i * 1024, vh[i], so);
        const unsigned sb = (ks * 7 + step - 16) * 64 * 1024 + nt * 256;
#pragma unroll
        for (int i = 0; i < 4; ++i) dma16(rd, dst + 16384 + i * 1024, vb[i], sb);
      }
    };
    issue(3);
    wait_vm<18>();
    bar();
    wait_vm<14>();
    bar();
    wait_vm<0>();
#pragma unroll 1
    for (int i = 1; i < 22; ++i) {
      if (i + 2 < 23) wait_vm<8>(); else wait_vm<0>();
      if (i == 10 && lw == 0 && !__all(*(const u64*)(shm + L_POLL + 1024 + lane * 16) == tag))
        spin_flags(flags + FLAG_B + (xcd + 8 * ((idx & ~3) + (lane & 3))) * 128, tag);
      bar();
      if (i + 3 < 23) issue(i + 3);
      if (i == 8 && lw == 0) dma16c(rfl, lds0 + L_POLL + 1024, (lane & 3) * 1024, FLAG_B + (xcd + 8 * (idx & ~3)) * 128);
    }
    if (id == 0 && lw == 1) {
#pragma unroll 1
      for (int q = 0; q < 4; ++q) spin_flags(flags + FLAG_A + (q * 64 + lane) * 128, tag);
      if (lane == 0) st_flag(flags + FLAG_E, tag);
    }
    return;
  }

  const int w2r = wid >> 1, w2c = wid & 1;
  const int t_out = mt * 128 + w2r * 64 + (ks & 1) * 32 + r;
  const int e_out = nt * 128 + w2c * 64 + (ks >> 1) * 32 + g * 4;
  f32x4 bc[4][4];
#pragma unroll
  for (int a = 0; a < 4; ++a)
#pragma unroll
    for (int b = 0; b < 4; ++b) bc[a][b] = f32x4{0.f, 0.f, 0.f, 0.f};
  int ac[2];
#pragma unroll
  for (int j = 0; j < 2; ++j) ac[j] = (w2r * 64 + r) * 128 + (((4 * j + g) ^ sw) << 4);
  int bd;
  {
    const int q = (lane & 15) >> 2, p = lane & 3;
    const int f = q | ((g & 1) << 2);
    bd = 16384 + (8 * g + q) * 256 + (((w2c * 4) ^ f) << 5) + p * 8;
  }
  bf16x8 fa[2][4], fb[2][4];
  auto sub = [&](int mb, const char* st, int j, bool doread) {
    const int nb = mb ^ 1;
    auto ra = [&](int b) { fa[nb][b] = *(const bf16x8*)(st + ac[j] + b * 2048); };
    auto rbf = [&](int b) { const int o = (bd ^ (b << 5)) + j * 8192; fb[nb][b] = tr_frag(st + o, st + o + 1024); };
#pragma unroll
    for (int ei = 0; ei < 4; ++ei) {
#pragma unroll
      for (int ti = 0; ti < 4; ++ti) {
        bc[ei][ti] = MFMA16(fb[mb][ei], fa[mb][ti], bc[ei][ti]);
        SB();
        if (doread) {
          const int n = ei * 4 + ti;
          if (n == 0) ra(0);
          if (n == 2) rbf(0);
          if (n == 4) ra(1);
          if (n == 6) ra(2);
          if (n == 8) ra(3);
          if (n == 10) rbf(1);
          if (n == 12) rbf(2);
          if (n == 14) rbf(3);
        }
        SB();
      }
    }
  };
  wait_vm<10>();
  bar();
  STAMP(0, 6);
#pragma unroll
  for (int i = 0; i < 4; ++i) {
    fa[0][i] = *(const bf16x8*)(shm + ac[0] + i * 2048);
    const int o = bd ^ (i << 5);
    fb[0][i] = tr_frag(shm + o, shm + o + 1024);
  }
  u32x2 xr[2][2];
#pragma unroll 1
  for (int i = 0; i < 22; ++i) {
    const char* cur = shm + (i & 3) * SLOT;
    const char* nxt = shm + ((i + 1) & 3) * SLOT;
    sub(0, cur, 1, true);
    if (i == 0) wait_vm<6>();
    if (i == 1) wait_vm<0>();
    bar();
    if (i == 1) {
      if (tid == 0) st_flag(flags + FLAG_B + id * 128, tag);
      if (wid < 2) dma4(mk_rsrc(wd, F * D * 2), lds0 + L_DUMMY, lane < 56 ? lane * 128 : 0x7fffff00, (ks * 448 + idx * 14 + wid * 7) * 1024);
    }
    sub(1, nxt, 0, true);
    if (i == 6) STAMP(0, 7);
    if (i == 15) STAMP(0, 8);
  }
#pragma unroll
  for (int ei = 0; ei < 2; ++ei)
#pragma unroll
    for (int ti = 0; ti < 2; ++ti) xr[ei][ti] = *(const u32x2*)(x + (size_t)(t_out + ti * 16) * D + e_out + ei * 16);
  sub(0, shm + (22 & 3) * SLOT, 1, true);
  sub(1, shm, 0, false);
  STAMP(0, 9);

  const int tile = mt * 4 + nt;
  const rsrc_t prs = mk_rsrc(ws + WS_P + (size_t)tile * 16 * 8192, 16 * 8192);
  f32x4 own[2][2];
#pragma unroll
  for (int q = 0; q < 4; ++q) {
    const int e2 = (q >> 1) * 2, t2 = (q & 1) * 2;
    if (q == ks) {
#pragma unroll
      for (int ei = 0; ei < 2; ++ei)
#pragma unroll
        for (int ti = 0; ti < 2; ++ti) own[ei][ti] = bc[e2 + ei][t2 + ti];
    } else {
      const int off = (q * 4 + ks) * 8192 + wid * 2048 + lane * 16;
#pragma unroll
      for (int ei = 0; ei < 2; ++ei) {
        const f32x4 a = bc[e2 + ei][t2], b = bc[e2 + ei][t2 + 1];
        u32x4 v = {pack2(a[0], a[1]), pack2(a[2], a[3]), pack2(b[0], b[1]), pack2(b[2], b[3])};
        __builtin_amdgcn_raw_buffer_store_b128(v, prs, off + ei * 1024, 0, 16);
      }
    }
  }
  wait_vm<0>();
  bar();
  STAMP(0, 10);
  if (wid == 0) {
    if (lane == 0) st_flag(flags + FLAG_X + (tile * 4 + ks) * 128, tag);
    spin_flags(flags + FLAG_X + (tile * 4 + ((ks + 1 + (lane % 3)) & 3)) * 128, tag);
  }
  bar();
  STAMP(0, 11);
  u32x4 pv[3][2];
#pragma unroll
  for (int i = 0; i < 3; ++i) {
    const int src = (ks + 1 + i) & 3;
    const int off = (ks * 4 + src) * 8192 + wid * 2048 + lane * 16;
    pv[i][0] = __builtin_amdgcn_raw_buffer_load_b128(prs, off, 0, 16);
    pv[i][1] = __builtin_amdgcn_raw_buffer_load_b128(prs, off + 1024, 0, 16);
  }
#pragma unroll
  for (int i = 0; i < 3; ++i)
#pragma unroll
    for (int ei = 0; ei < 2; ++ei)
#pragma unroll
      for (int ti = 0; ti < 2; ++ti) {
        own[ei][ti][0] += bflo(pv[i][ei][2 * ti]);
        own[ei][ti][1] += bfhi(pv[i][ei][2 * ti]);
        own[ei][ti][2] += bflo(pv[i][ei][2 * ti + 1]);
        own[ei][ti][3] += bfhi(pv[i][ei][2 * ti + 1]);
      }
#pragma unroll
  for (int ti = 0; ti < 2; ++ti) {
    unsigned o[2][2];
#pragma unroll
    for (int ei = 0; ei < 2; ++ei) {
      o[ei][0] = pack2(own[ei][ti][0] + bflo(xr[ei][ti][0]), own[ei][ti][1] + bfhi(xr[ei][ti][0]));
      o[ei][1] = pack2(own[ei][ti][2] + bflo(xr[ei][ti][1]), own[ei][ti][3] + bfhi(xr[ei][ti][1]));
    }
    const u32x4 v = swap16(o[0][0], o[0][1], o[1][0], o[1][1]);
    const int e0 = nt * 128 + w2c * 64 + (ks >> 1) * 32 + (g & 1) * 16 + (g >> 1) * 8;
    *(u32x4*)(out + (size_t)(t_out + ti * 16) * D + e0) = v;
  }
  STAMP(0, 12);
}

extern "C" void kernel_launch(void* const* d_in, const int* in_sizes, int n_in, void* d_out, int out_size,
                              void* d_ws, size_t ws_size, hipStream_t stream) {
  k_fused<<<256, 512, 0, stream>>>((const u16*)d_in[0], (const u16*)d_in[1], (const u16*)d_in[2], (const u16*)d_in[3],
                                   (const u16*)d_in[4], (const u16*)d_in[6], (const u16*)d_in[7], (const u16*)d_in[8],
                                   (char*)d_ws, (u16*)d_out);
}
```

```cpp
#define PROBE_A 9
#define PROBE_B 12
#include <hip/hip_runtime.h>
#include <hip/hip_bf16.h>
#include <stdint.h>

typedef unsigned short u16;
typedef unsigned long long u64;
typedef __attribute__((ext_vector_type(8))) short bf16x8;
typedef __attribute__((ext_vector_type(4))) short s16x4;
typedef __attribute__((ext_vector_type(4))) float f32x4;
typedef __attribute__((ext_vector_type(2))) float f32x2;
typedef __attribute__((ext_vector_type(2))) __bf16 bf2;
typedef __attribute__((ext_vector_type(4))) unsigned u32x4;
typedef __attribute__((ext_vector_type(2))) unsigned u32x2;
typedef __amdgpu_buffer_rsrc_t rsrc_t;
#define LDSP(T, p) ((__attribute__((address_space(3))) T*)(p))
#define SB() __builtin_amdgcn_sched_barrier(0)
#define DI __device__ __forceinline__
#define MFMA16(a, b, c) __builtin_amdgcn_mfma_f32_16x16x32_bf16(a, b, c, 0, 0, 0)

constexpr int S = 2048, D = 512, F = 1792;
constexpr size_t WS_AB = 0;
constexpr size_t WS_H = WS_AB + (size_t)4096 * 512 * 2;
constexpr size_t WS_P = WS_H + (size_t)28 * 2048 * 128;
constexpr size_t WS_FLAG = WS_P + (size_t)64 * 16 * 8192;
constexpr int FLAG_A = 0, FLAG_B = 256 * 128, FLAG_X = 512 * 128, FLAG_E = 768 * 128;
constexpr int CODE_BYTES = 23552;
constexpr int SLOT = 36864;
constexpr int L_STAT = 4 * SLOT;
constexpr int L_CS1 = L_STAT + 1024;
constexpr int L_CS2 = L_STAT + 3072;
constexpr int L_DUMMY = L_STAT + 5120;
constexpr int L_POLL = L_STAT + 6144;
constexpr int LDS_BYTES = L_STAT + 8192;

template <int N> DI void wait_vm() { asm volatile("s_waitcnt vmcnt(%0)" ::"n"(N) : "memory"); }
DI void wait_lgkm0() { asm volatile("s_waitcnt lgkmcnt(0)" ::: "memory"); }
DI void bar() {
  asm volatile("" ::: "memory");
  __builtin_amdgcn_s_barrier();
  asm volatile("" ::: "memory");
}
DI unsigned pack2(float a, float b) {
  f32x2 p = {a, b};
  return __builtin_bit_cast(unsigned, __builtin_convertvector(p, bf2));
}
DI float bflo(unsigned u) { return __builtin_bit_cast(float, u << 16); }
DI float bfhi(unsigned u) { return __builtin_bit_cast(float, u & 0xffff0000u); }
DI rsrc_t mk_rsrc(const void* p, unsigned bytes) { return __builtin_amdgcn_make_buffer_rsrc((void*)p, 0, bytes, 0x00020000); }
DI void dma16(rsrc_t r, unsigned lds, unsigned voff, unsigned soff) {
  asm volatile("s_mov_b32 m0, %0\n\ts_nop 0\n\tbuffer_load_dwordx4 %1, %2, %3 offen lds" ::"s"(lds), "v"(voff), "s"(r), "s"(soff) : "memory");
}
DI void dma16c(rsrc_t r, unsigned lds, unsigned voff, unsigned soff) {
  asm volatile("s_mov_b32 m0, %0\n\ts_nop 0\n\tbuffer_load_dwordx4 %1, %2, %3 offen sc1 lds" ::"s"(lds), "v"(voff), "s"(r), "s"(soff) : "memory");
}
DI void dma4(rsrc_t r, unsigned lds, unsigned voff, unsigned soff) {
  asm volatile("s_mov_b32 m0, %0\n\ts_nop 0\n\tbuffer_load_dword %1, %2, %3 offen lds" ::"s"(lds), "v"(voff), "s"(r), "s"(soff) : "memory");
}
DI void dma16_lo32(rsrc_t r, unsigned lds, unsigned voff, unsigned soff) {
  asm volatile("s_mov_b32 m0, %0\n\ts_mov_b32 exec_hi, 0\n\tbuffer_load_dwordx4 %1, %2, %3 offen lds\n\ts_mov_b32 exec_hi, -1"
               ::"s"(lds), "v"(voff), "s"(r), "s"(soff) : "memory");
}
DI void dma4_lo8(rsrc_t r, unsigned lds, unsigned voff, unsigned soff) {
  asm volatile("s_mov_b32 m0, %0\n\ts_mov_b64 exec, 0xff\n\tbuffer_load_dword %1, %2, %3 offen lds\n\ts_mov_b64 exec, -1"
               ::"s"(lds), "v"(voff), "s"(r), "s"(soff) : "memory");
}
DI unsigned opq(unsigned v) { asm volatile("" : "+v"(v)); return v; }
DI bf16x8 rd128(unsigned a) { return *(const __attribute__((address_space(3))) bf16x8*)(size_t)a; }
DI bf16x8 rdtr(unsigned a0, unsigned a1) {
  s16x4 a = __builtin_amdgcn_ds_read_tr16_b64_v4i16((__attribute__((address_space(3))) s16x4*)(size_t)a0);
  s16x4 b = __builtin_amdgcn_ds_read_tr16_b64_v4i16((__attribute__((address_space(3))) s16x4*)(size_t)a1);
  return __builtin_shufflevector(a, b, 0, 1, 2, 3, 4, 5, 6, 7);
}
DI bf16x8 tr_frag(const char* p0, const char* p1) {
  s16x4 a = __builtin_amdgcn_ds_read_tr16_b64_v4i16(LDSP(s16x4, p0));
  s16x4 b = __builtin_amdgcn_ds_read_tr16_b64_v4i16(LDSP(s16x4, p1));
  return __builtin_shufflevector(a, b, 0, 1, 2, 3, 4, 5, 6, 7);
}
DI float gelu_mul(float g, float u) {
  float z = g * (-2.302208198f - 0.1029432397f * g * g);
  float e = __builtin_amdgcn_exp2f(z);
  return g * u * __builtin_amdgcn_rcpf(1.0f + e);
}
DI float dot2(unsigned a, unsigned b, float acc) {
  return __builtin_amdgcn_fdot2_f32_bf16(__builtin_bit_cast(bf2, a), __builtin_bit_cast(bf2, b), acc, false);
}
DI float frag_sum(bf16x8 f, float acc) {
  const u32x4 u = __builtin_bit_cast(u32x4, f);
#pragma unroll
  for (int i = 0; i < 4; ++i) { const unsigned w = u[i]; acc = dot2(w, 0x3f803f80u, acc); }
  return acc;
}
DI float frag_sq(bf16x8 f, float acc) {
  const u32x4 u = __builtin_bit_cast(u32x4, f);
#pragma unroll
  for (int i = 0; i < 4; ++i) { const unsigned w = u[i]; acc = dot2(w, w, acc); }
  return acc;
}
DI float red_g(float v) {
  v += __shfl_xor(v, 16);
  v += __shfl_xor(v, 32);
  return v;
}
DI u32x4 swap16(unsigned a0, unsigned a1, unsigned b0, unsigned b1) {
  auto s0 = __builtin_amdgcn_permlane16_swap(a0, b0, false, false);
  auto s1 = __builtin_amdgcn_permlane16_swap(a1, b1, false, false);
  return u32x4{s0[0], s1[0], s0[1], s1[1]};
}
DI u64 ld_flag(const char* p) { return __hip_atomic_load((const u64*)p, __ATOMIC_RELAXED, __HIP_MEMORY_SCOPE_AGENT); }
DI void st_flag(char* p, u64 v) { __hip_atomic_store((u64*)p, v, __ATOMIC_RELAXED, __HIP_MEMORY_SCOPE_AGENT); }
DI void spin_flags(const char* p, u64 tag) {
#pragma unroll 1
  for (unsigned spin = 0; spin < (1u << 20); ++spin) {
    if (__all(ld_flag(p) == tag)) break;
    __builtin_amdgcn_s_sleep(1);
  }
}

#define STAMP(k, i) do { if ((i) == PROBE_A) tpa = __builtin_amdgcn_s_memrealtime(); if ((i) == PROBE_B) tpb = __builtin_amdgcn_s_memrealtime(); } while (0)

constexpr int st_ops(int stage) { return stage < 8 ? 5 : (stage < 12 ? 4 : 0); }
constexpr int pf_ops(int n) { return (n >= 0 && n < 8) ? 1 : 0; }
constexpr int allow(int n) { return pf_ops(n - 3) + st_ops(n + 1) + pf_ops(n - 2) + st_ops(n + 2) + pf_ops(n - 1); }

__global__ __launch_bounds__(512, 2) void k_fused(const u16* __restrict__ x, const u16* __restrict__ cre,
                                                  const u16* __restrict__ cim, const u16* __restrict__ wre,
                                                  const u16* __restrict__ wim, const u16* __restrict__ wg,
                                                  const u16* __restrict__ wu, const u16* __restrict__ wd,
                                                  char* __restrict__ ws, u16* __restrict__ out) {
  __shared__ __attribute__((aligned(1024))) char shm[LDS_BYTES];
  const unsigned lds0 = (unsigned)(size_t)LDSP(char, shm);
  const int tid = threadIdx.x, lane = tid & 63;
  const int wid = __builtin_amdgcn_readfirstlane(tid >> 6);
  const int id = blockIdx.x, xcd = id & 7, idx = id >> 3;
  const int c = xcd >> 1, cgl = idx & 3;
  const int rb = (xcd & 1) * 8 + (idx >> 2);
  const bool isQ = cgl == 3;
  const int unit1 = 7 * c + 4 + cgl, unit2 = 7 * c + cgl;
  const int rot = ((idx >> 2) + 2 * cgl) & 7;
  const int r = lane & 15, g = lane >> 4;
  const int wr = wid & 3, gq = wid >> 2;
  char* flags = ws + WS_FLAG;
  u64 tpa = 0, tpb = 0;
  if (wid == 7) {
    const u64 pc = __builtin_amdgcn_s_getpc() & ~(u64)127;
    dma4(mk_rsrc((const void*)pc, CODE_BYTES), lds0 + L_DUMMY, lane * 512, cgl * 128);
  }
  STAMP(0, 0);

  const rsrc_t rx = mk_rsrc(x, S * D * 2);
  const rsrc_t rre = mk_rsrc(wre, D * D * 2);
  const rsrc_t rw = mk_rsrc(gq ? wu : wg, D * F * 2);
  const rsrc_t r1 = isQ ? mk_rsrc(wim, D * D * 2) : rw;
  const rsrc_t rc = mk_rsrc((c & 2) ? cim : cre, S * S * 2);
  unsigned vrow[2], vw[2], v1[2];
#pragma unroll
  for (int i = 0; i < 2; ++i) {
    const int row = wid * 16 + i * 8 + (lane >> 3);
    vrow[i] = row * 1024 + (((lane & 7) ^ ((row >> 1) & 7)) << 4);
    const int kr = wr * 16 + i * 8 + (lane >> 3);
    const int f = ((kr >> 1) & 1) | (((kr >> 3) & 1) << 1);
    vw[i] = kr * (F * 2) + (((lane & 7) ^ (f << 1)) << 4);
    v1[i] = isQ ? vrow[i] : vw[i];
  }
  const unsigned vd32 = (wid * 4 + (lane >> 3)) * 1024 + (((lane & 7) ^ (((wid * 4 + (lane >> 3)) >> 1) & 7)) << 4);
  const unsigned base1 = isQ ? c * 131072 : unit1 * 128, step1 = isQ ? 128 : 64 * F * 2;
  auto issueA = [&](int stage) {
    const unsigned sl = lds0 + (stage & 3) * SLOT + wid * 2048;
    if (stage < 8) {
      const int kp = (stage + rot) & 7;
      dma16(rx, sl, vrow[0], rb * 131072 + kp * 128);
      dma16(rx, sl + 1024, vrow[1], rb * 131072 + kp * 128);
      dma16(r1, sl + 16384, v1[0], base1 + kp * step1);
      dma16(r1, sl + 16384 + 1024, v1[1], base1 + kp * step1);
      dma16_lo32(rre, lds0 + (stage & 3) * SLOT + 32768 + wid * 512, vd32, (c * 4 + cgl) * 32768 + kp * 128);
    } else {
#pragma unroll
      for (int h = 0; h < 2; ++h) {
        const int kp = ((stage - 8) * 2 + h + rot) & 7;
        dma16(rw, sl + h * 16384, vw[0], unit2 * 128 + kp * (64 * F * 2));
        dma16(rw, sl + h * 16384 + 1024, vw[1], unit2 * 128 + kp * (64 * F * 2));
      }
    }
  };
  issueA(0);
  issueA(1);
  issueA(2);
  u64 tag = *(const __attribute__((address_space(4))) u64*)(flags + FLAG_E) + 1;
  asm volatile("" : "+s"(tag));
  const rsrc_t rfl = mk_rsrc(flags, FLAG_E + 128);
  auto prefetchC = [&](int n) {
    dma4_lo8(rc, lds0 + L_DUMMY, lane * 128, (rb * 128 + cgl * 32 + wid * 4 + (n >> 1)) * (S * 2) + (c & 1) * 2048 + (n & 1) * 1024);
  };

  const int sw = (r >> 1) & 7;
  const int jl = wr >> 1, bl = wr & 1;
  const unsigned xa = lds0 + r * 128 + (((4 * jl + g) ^ sw) << 4);
  unsigned wa;
  {
    const int q = (lane & 15) >> 2, p = lane & 3;
    const int f = (q >> 1) | ((g & 1) << 1);
    wa = lds0 + (8 * g + q) * 128 + (((2 * gq + bl) ^ f) << 5) + p * 8 + jl * 4096;
  }
  bf16x8 xf[8][2][2];
  bf16x8 wf[4][2], df[2][2];
  f32x4 acc[4][2], a0[2];
  float ssum = 0.f, ssq = 0.f, cs0 = 0.f, cs1 = 0.f, csd = 0.f;
#pragma unroll
  for (int a = 0; a < 4; ++a) { acc[a][0] = f32x4{0.f, 0.f, 0.f, 0.f}; acc[a][1] = f32x4{0.f, 0.f, 0.f, 0.f}; }
  a0[0] = f32x4{0.f, 0.f, 0.f, 0.f}; a0[1] = f32x4{0.f, 0.f, 0.f, 0.f};

  auto read_w_tr = [&](unsigned st) {
    const unsigned w0 = opq(wa);
#pragma unroll
    for (int j = 0; j < 2; ++j)
#pragma unroll
      for (int f1 = 0; f1 < 2; ++f1) {
        const unsigned o = (w0 ^ (f1 << 5) ^ (j << 12)) + st;
        wf[f1][j] = rdtr(o, o + 512);
        wf[2 + f1][j] = rdtr(o + 8192, o + 8192 + 512);
      }
  };
  auto ffn_epi = [&](int csoff, int unit) {
    const rsrc_t rh = mk_rsrc(ws + WS_H, 28 * S * 128);
    f32x4 cg4[2], cu4[2];
#pragma unroll
    for (int fi = 0; fi < 2; ++fi) {
      const int col = gq * 32 + (fi ^ bl) * 16 + 4 * g;
      cg4[fi] = *(const f32x4*)(shm + csoff + col * 4) + *(const f32x4*)(shm + csoff + 640 + col * 4);
      cu4[fi] = *(const f32x4*)(shm + csoff + (64 + col) * 4) + *(const f32x4*)(shm + csoff + 640 + (64 + col) * 4);
    }
#pragma unroll
    for (int b = 0; b < 2; ++b) {
      const int row = wr * 32 + (b ^ gq) * 16 + r;
      const f32x2 st = *(const f32x2*)(shm + L_STAT + row * 8);
      unsigned o[2][2];
#pragma unroll
      for (int fi = 0; fi < 2; ++fi) {
        float h[4];
#pragma unroll
        for (int q = 0; q < 4; ++q)
          h[q] = gelu_mul(acc[fi][b][q] * st[0] - st[1] * cg4[fi][q], acc[2 + fi][b][q] * st[0] - st[1] * cu4[fi][q]);
        o[fi][0] = pack2(h[0], h[1]);
        o[fi][1] = pack2(h[2], h[3]);
      }
      const u32x4 v = swap16(o[0][0], o[0][1], o[1][0], o[1][1]);
      const int chunk = gq * 4 + ((g & 1) ^ bl) * 2 + (g >> 1);
      __builtin_amdgcn_raw_buffer_store_b128(v, rh, (unit * S + rb * 128 + row) * 128 + chunk * 16, 0, 16);
    }
  };
  auto dft_epi = [&](f32x4 v0, f32x4 v1_, int csidx, int row, int grow, int e0, float sgn) {
    const rsrc_t rab = mk_rsrc(ws + WS_AB, 4096 * D * 2);
    f32x2 st = *(const f32x2*)(shm + L_STAT + row * 8);
    st *= sgn;
    const int i0 = csidx + bl * 16 + 4 * g, i1 = csidx + (bl ^ 1) * 16 + 4 * g;
    const f32x4 c0 = *(const f32x4*)(shm + L_CS1 + i0 * 4) + *(const f32x4*)(shm + L_CS1 + 640 + i0 * 4);
    const f32x4 c1 = *(const f32x4*)(shm + L_CS1 + i1 * 4) + *(const f32x4*)(shm + L_CS1 + 640 + i1 * 4);
    const f32x4 y0 = v0 * st[0] - c0 * st[1], y1 = v1_ * st[0] - c1 * st[1];
    const u32x4 v = swap16(pack2(y0[0], y0[1]), pack2(y0[2], y0[3]), pack2(y1[0], y1[1]), pack2(y1[2], y1[3]));
    const int chunk = ((g & 1) ^ bl) * 2 + (g >> 1);
    __builtin_amdgcn_raw_buffer_store_b128(v, rab, (grow * D + e0) * 2 + chunk * 16, 0, 16);
  };

#pragma unroll
  for (int p = 0; p < 8; ++p) {
    if (p == 0) { wait_vm<allow(0)>(); bar(); STAMP(0, 1); }
    else if (gq == 0) {
      switch (p) {
        case 1: wait_vm<allow(1)>(); break; case 2: wait_vm<allow(2)>(); break; case 3: wait_vm<allow(3)>(); break;
        case 4: wait_vm<allow(4)>(); break; case 5: wait_vm<allow(5)>(); break; case 6: wait_vm<allow(6)>(); break;
        default: wait_vm<allow(7)>(); break;
      }
      bar();
    }
    const unsigned st = (p & 3) * SLOT;
    {
      const unsigned x0 = opq(xa);
#pragma unroll
      for (int j = 0; j < 2; ++j) {
        const unsigned xj = x0 ^ (j << 6);
#pragma unroll
        for (int b = 0; b < 2; ++b) xf[p][b][j] = rd128(xj + st + (wr * 32 + (b ^ gq) * 16) * 128);
#pragma unroll
        for (int eb = 0; eb < 2; ++eb) df[eb][j] = rd128(xj + st + 32768 + (eb ^ bl) * 2048);
      }
      if (isQ) {
#pragma unroll
        for (int j = 0; j < 2; ++j)
#pragma unroll
          for (int f1 = 0; f1 < 2; ++f1) {
            const unsigned o = (x0 ^ (j << 6)) + st + 16384 + gq * 8192 + (f1 ^ bl) * 2048;
            wf[f1][j] = rd128(o);
            wf[2 + f1][j] = rd128(o + 4096);
          }
      } else {
        read_w_tr(st + 16384);
      }
    }
    issueA(p + 3);
    prefetchC(p);
    auto sums = [&]() {
      ssum = frag_sum(xf[p][0][0], ssum); ssq = frag_sq(xf[p][0][0], ssq);
      ssum = frag_sum(xf[p][0][1], ssum); ssq = frag_sq(xf[p][0][1], ssq);
      cs0 = frag_sum(wf[0][0], cs0); cs1 = frag_sum(wf[2][0], cs1); csd = frag_sum(df[0][0], csd);
      asm volatile("" : "+v"(ssum), "+v"(ssq), "+v"(cs0), "+v"(cs1), "+v"(csd));
    };
    if (p == 7) {
      sums();
      ssum = red_g(ssum); ssq = red_g(ssq); cs0 = red_g(cs0); cs1 = red_g(cs1); csd = red_g(csd);
      const float m = ssum * (1.0f / D);
      const float var = fmaxf(ssq * (1.0f / D) - m * m, 0.f);
      const float rs = __builtin_amdgcn_rsqf(var + 1e-5f);
      if (g == 0) *(f32x2*)(shm + L_STAT + (wr * 32 + gq * 16 + r) * 8) = f32x2{rs, m * rs};
      float* cs = (float*)(shm + L_CS1 + jl * 640);
      if (g == 1) cs[(isQ ? gq * 64 : gq * 32) + bl * 16 + r] = cs0;
      if (g == 2) cs[(isQ ? gq * 64 + 32 : 64 + gq * 32) + bl * 16 + r] = cs1;
      if (g == 3) cs[128 + bl * 16 + r] = csd;
      cs0 = 0.f; cs1 = 0.f;
    }
    SB();
    if (gq == 1) {
      wait_lgkm0();
      switch (p) {
        case 0: wait_vm<allow(1)>(); break; case 1: wait_vm<allow(2)>(); break; case 2: wait_vm<allow(3)>(); break;
        case 3: wait_vm<allow(4)>(); break; case 4: wait_vm<allow(5)>(); break; case 5: wait_vm<allow(6)>(); break;
        case 6: wait_vm<allow(7)>(); break; default: wait_vm<allow(8)>(); break;
      }
      bar();
    }
    SB();
#pragma unroll
    for (int j = 0; j < 2; ++j) {
#pragma unroll
      for (int fi = 0; fi < 4; ++fi)
#pragma unroll
        for (int b = 0; b < 2; ++b) acc[fi][b] = MFMA16(wf[fi][j], xf[p][b][j], acc[fi][b]);
#pragma unroll
      for (int eb = 0; eb < 2; ++eb) a0[eb] = MFMA16(df[eb][j], xf[p][0][j], a0[eb]);
    }
    if (p < 7) sums();
    SB();
  }
  if (gq == 0) { wait_lgkm0(); wait_vm<allow(8)>(); bar(); }
  STAMP(0, 2);
  dft_epi(a0[0], a0[1], 128, wr * 32 + gq * 16 + r, rb * 128 + wr * 32 + gq * 16 + r, (c * 4 + cgl) * 32, 1.f);
  if (isQ) {
#pragma unroll
    for (int b = 0; b < 2; ++b)
#pragma unroll
      for (int h = 0; h < 2; ++h) {
        const int row = wr * 32 + (b ^ gq) * 16 + r;
        dft_epi(acc[2 * h][b], acc[2 * h + 1][b], gq * 64 + h * 32, row, S + rb * 128 + row, c * 128 + gq * 64 + h * 32, -1.f);
      }
  } else {
    ffn_epi(L_CS1, unit1);
  }
#pragma unroll
  for (int a = 0; a < 4; ++a) { acc[a][0] = f32x4{0.f, 0.f, 0.f, 0.f}; acc[a][1] = f32x4{0.f, 0.f, 0.f, 0.f}; }
  STAMP(0, 3);

  const rsrc_t rab = mk_rsrc(ws + WS_AB, 4096 * D * 2);
  unsigned vb2[2], vc2[2];
#pragma unroll
  for (int i = 0; i < 2; ++i) {
    const int row = wid * 16 + i * 8 + (lane >> 3);
    vc2[i] = row * (S * 2) + (((lane & 7) ^ ((row >> 1) & 7)) << 4);
    const int kr = wid * 8 + i * 4 + (lane >> 4);
    const int f = (kr & 3) | (((kr >> 3) & 1) << 2);
    vb2[i] = kr * 1024 + (((lane & 15) ^ (f << 1)) << 4);
  }
  auto issueB0 = [&](int step) {
    const unsigned sl = lds0 + step * SLOT + wid * 2048;
    const unsigned sc = rb * 128 * (S * 2) + ((c & 1) * 16 + step) * 128;
    dma16(rc, sl, vc2[0], sc);
    dma16(rc, sl + 1024, vc2[1], sc);
    const unsigned sb = (c * 1024 + step * 64) * 1024 + cgl * 256;
    dma16c(rab, sl + 16384, vb2[0], sb);
    dma16c(rab, sl + 16384 + 1024, vb2[1], sb);
  };
#pragma unroll
  for (int p = 0; p < 8; ++p) {
    const int n = 8 + (p >> 1);
    if ((p & 1) == 0 && p > 0 && gq == 0) {
      wait_vm<0>();
      if (p == 6 && wid == 0 && !__all(*(const u64*)(shm + L_POLL + lane * 16) == tag)) spin_flags(flags + FLAG_A + ((2 * cgl + (c & 1)) + 8 * (lane & 31)) * 128, tag);
      bar();
      if (p == 2 && tid == 0) st_flag(flags + FLAG_A + id * 128, tag);
    }
    read_w_tr((n & 3) * SLOT + (p & 1) * 16384);
    if (p == 0) issueA(11);
    if (p == 4 && wid == 0) dma16c(rfl, lds0 + L_POLL, (lane & 31) * 1024, FLAG_A + (2 * cgl + (c & 1)) * 128);
    if (p >= 6) issueB0(p - 6);
    if (p == 7) {
      cs0 = frag_sum(wf[0][0], cs0); cs1 = frag_sum(wf[2][0], cs1);
      cs0 = red_g(cs0); cs1 = red_g(cs1);
      float* cs = (float*)(shm + L_CS2 + jl * 640);
      if (g == 1) cs[gq * 32 + bl * 16 + r] = cs0;
      if (g == 2) cs[64 + gq * 32 + bl * 16 + r] = cs1;
    }
    SB();
    if ((p & 1) == 1 && gq == 1) {
      wait_lgkm0();
      if (p < 7) wait_vm<0>();
      bar();
    }
    SB();
#pragma unroll
    for (int j = 0; j < 2; ++j)
#pragma unroll
      for (int fi = 0; fi < 4; ++fi)
#pragma unroll
        for (int b = 0; b < 2; ++b) acc[fi][b] = MFMA16(wf[fi][j], xf[p][b][j], acc[fi][b]);
    if (p < 7) { cs0 = frag_sum(wf[0][0], cs0); cs1 = frag_sum(wf[2][0], cs1); asm volatile("" : "+v"(cs0), "+v"(cs1)); }
    SB();
  }
  if (gq == 0) { wait_lgkm0(); bar(); }
  STAMP(0, 4);
  issueB0(2);
  ffn_epi(L_CS2, unit2);
  STAMP(0, 5);

  const int ks = c, mt = rb, nt = cgl;
  if (gq == 1) {
    const int lw = wr;
    const rsrc_t rh = mk_rsrc(ws + WS_H, 28 * S * 128);
    const rsrc_t rd = mk_rsrc(wd, F * D * 2);
    unsigned va[4], vh[4], vb[4];
#pragma unroll
    for (int i = 0; i < 4; ++i) {
      const int row = lw * 32 + i * 8 + (lane >> 3);
      const int ch = ((lane & 7) ^ ((row >> 1) & 7)) << 4;
      va[i] = row * (S * 2) + ch;
      vh[i] = row * 128 + ch;
      const int kr = lw * 16 + i * 4 + (lane >> 4);
      const int f = (kr & 3) | (((kr >> 3) & 1) << 2);
      vb[i] = kr * 1024 + (((lane & 15) ^ (f << 1)) << 4);
    }
    auto issue = [&](int step) {
      const unsigned dst = lds0 + (step & 3) * SLOT + lw * 4096;
      if (step < 16) {
        const unsigned so = mt * 128 * (S * 2) + ((ks & 1) * 16 + step) * 128;
#pragma unroll
        for (int i = 0; i < 4; ++i) dma16(rc, dst + i * 1024, va[i], so);
        const unsigned sb = (ks * 1024 + step * 64) * 1024 + nt * 256;
#pragma unroll
        for (int i = 0; i < 4; ++i) dma16c(rab, dst + 16384 + i * 1024, vb[i], sb);
      } else {
        const unsigned so = ((ks * 7 + step - 16) * S + mt * 128) * 128;
#pragma unroll
        for (int i = 0; i < 4; ++i) dma16c(rh, dst + i * 1024, vh[i], so);
        const unsigned sb = (ks * 7 + step - 16) * 64 * 1024 + nt * 256;
#pragma unroll
        for (int i = 0; i < 4; ++i) dma16(rd, dst + 16384 + i * 1024, vb[i], sb);
      }
    };
    issue(3);
    wait_vm<18>();
    bar();
    wait_vm<14>();
    bar();
    wait_vm<0>();
#pragma unroll 1
    for (int i = 1; i < 22; ++i) {
      if (i + 2 < 23) wait_vm<8>(); else wait_vm<0>();
      if (i == 10 && lw == 0 && !__all(*(const u64*)(shm + L_POLL + 1024 + lane * 16) == tag))
        spin_flags(flags + FLAG_B + (xcd + 8 * ((idx & ~3) + (lane & 3))) * 128, tag);
      bar();
      if (i + 3 < 23) issue(i + 3);
      if (i == 8 && lw == 0) dma16c(rfl, lds0 + L_POLL + 1024, (lane & 3) * 1024, FLAG_B + (xcd + 8 * (idx & ~3)) * 128);
    }
    if (id == 0 && lw == 1) {
#pragma unroll 1
      for (int q = 0; q < 4; ++q) spin_flags(flags + FLAG_A + (q * 64 + lane) * 128, tag);
      if (lane == 0) st_flag(flags + FLAG_E, tag);
    }
    return;
  }

  const int w2r = wid >> 1, w2c = wid & 1;
  const int t_out = mt * 128 + w2r * 64 + (ks & 1) * 32 + r;
  const int e_out = nt * 128 + w2c * 64 + (ks >> 1) * 32 + g * 4;
  f32x4 bc[4][4];
#pragma unroll
  for (int a = 0; a < 4; ++a)
#pragma unroll
    for (int b = 0; b < 4; ++b) bc[a][b] = f32x4{0.f, 0.f, 0.f, 0.f};
  int ac[2];
#pragma unroll
  for (int j = 0; j < 2; ++j) ac[j] = (w2r * 64 + r) * 128 + (((4 * j + g) ^ sw) << 4);
  int bd;
  {
    const int q = (lane & 15) >> 2, p = lane & 3;
    const int f = q | ((g & 1) << 2);
    bd = 16384 + (8 * g + q) * 256 + (((w2c * 4) ^ f) << 5) + p * 8;
  }
  bf16x8 fa[2][4], fb[2][4];
  auto sub = [&](int mb, const char* st, int j, bool doread) {
    const int nb = mb ^ 1;
    auto ra = [&](int b) { fa[nb][b] = *(const bf16x8*)(st + ac[j] + b * 2048); };
    auto rbf = [&](int b) { const int o = (bd ^ (b << 5)) + j * 8192; fb[nb][b] = tr_frag(st + o, st + o + 1024); };
#pragma unroll
    for (int ei = 0; ei < 4; ++ei) {
#pragma unroll
      for (int ti = 0; ti < 4; ++ti) {
        bc[ei][ti] = MFMA16(fb[mb][ei], fa[mb][ti], bc[ei][ti]);
        SB();
        if (doread) {
          const int n = ei * 4 + ti;
          if (n == 0) ra(0);
          if (n == 2) rbf(0);
          if (n == 4) ra(1);
          if (n == 6) ra(2);
          if (n == 8) ra(3);
          if (n == 10) rbf(1);
          if (n == 12) rbf(2);
          if (n == 14) rbf(3);
        }
        SB();
      }
    }
  };
  wait_vm<10>();
  bar();
  STAMP(0, 6);
#pragma unroll
  for (int i = 0; i < 4; ++i) {
    fa[0][i] = *(const bf16x8*)(shm + ac[0] + i * 2048);
    const int o = bd ^ (i << 5);
    fb[0][i] = tr_frag(shm + o, shm + o + 1024);
  }
  u32x2 xr[2][2];
#pragma unroll 1
  for (int i = 0; i < 22; ++i) {
    const char* cur = shm + (i & 3) * SLOT;
    const char* nxt = shm + ((i + 1) & 3) * SLOT;
    sub(0, cur, 1, true);
    if (i == 0) wait_vm<6>();
    if (i == 1) wait_vm<0>();
    bar();
    if (i == 1) {
      if (tid == 0) st_flag(flags + FLAG_B + id * 128, tag);
      if (wid < 2) dma4(mk_rsrc(wd, F * D * 2), lds0 + L_DUMMY, lane < 56 ? lane * 128 : 0x7fffff00, (ks * 448 + idx * 14 + wid * 7) * 1024);
    }
    sub(1, nxt, 0, true);
    if (i == 6) STAMP(0, 7);
    if (i == 15) STAMP(0, 8);
  }
#pragma unroll
  for (int ei = 0; ei < 2; ++ei)
#pragma unroll
    for (int ti = 0; ti < 2; ++ti) xr[ei][ti] = *(const u32x2*)(x + (size_t)(t_out + ti * 16) * D + e_out + ei * 16);
  sub(0, shm + (22 & 3) * SLOT, 1, true);
  sub(1, shm, 0, false);
  STAMP(0, 9);

  const int tile = mt * 4 + nt;
  const rsrc_t prs = mk_rsrc(ws + WS_P + (size_t)tile * 16 * 8192, 16 * 8192);
  f32x4 own[2][2];
#pragma unroll
  for (int q = 0; q < 4; ++q) {
    const int e2 = (q >> 1) * 2, t2 = (q & 1) * 2;
    if (q == ks) {
#pragma unroll
      for (int ei = 0; ei < 2; ++ei)
#pragma unroll
        for (int ti = 0; ti < 2; ++ti) own[ei][ti] = bc[e2 + ei][t2 + ti];
    } else {
      const int off = (q * 4 + ks) * 8192 + wid * 2048 + lane * 16;
#pragma unroll
      for (int ei = 0; ei < 2; ++ei) {
        const f32x4 a = bc[e2 + ei][t2], b = bc[e2 + ei][t2 + 1];
        u32x4 v = {pack2(a[0], a[1]), pack2(a[2], a[3]), pack2(b[0], b[1]), pack2(b[2], b[3])};
        __builtin_amdgcn_raw_buffer_store_b128(v, prs, off + ei * 1024, 0, 16);
      }
    }
  }
  wait_vm<0>();
  bar();
  STAMP(0, 10);
  if (wid == 0) {
    if (lane == 0) st_flag(flags + FLAG_X + (tile * 4 + ks) * 128, tag);
    spin_flags(flags + FLAG_X + (tile * 4 + ((ks + 1 + (lane % 3)) & 3)) * 128, tag);
  }
  bar();
  STAMP(0, 11);
  u32x4 pv[3][2];
#pragma unroll
  for (int i = 0; i < 3; ++i) {
    const int src = (ks + 1 + i) & 3;
    const int off = (ks * 4 + src) * 8192 + wid * 2048 + lane * 16;
    pv[i][0] = __builtin_amdgcn_raw_buffer_load_b128(prs, off, 0, 16);
    pv[i][1] = __builtin_amdgcn_raw_buffer_load_b128(prs, off + 1024, 0, 16);
  }
#pragma unroll
  for (int i = 0; i < 3; ++i)
#pragma unroll
    for (int ei = 0; ei < 2; ++ei)
#pragma unroll
      for (int ti = 0; ti < 2; ++ti) {
        own[ei][ti][0] += bflo(pv[i][ei][2 * ti]);
        own[ei][ti][1] += bfhi(pv[i][ei][2 * ti]);
        own[ei][ti][2] += bflo(pv[i][ei][2 * ti + 1]);
        own[ei][ti][3] += bfhi(pv[i][ei][2 * ti + 1]);
      }
#pragma unroll
  for (int ti = 0; ti < 2; ++ti) {
    unsigned o[2][2];
#pragma unroll
    for (int ei = 0; ei < 2; ++ei) {
      o[ei][0] = pack2(own[ei][ti][0] + bflo(xr[ei][ti][0]), own[ei][ti][1] + bfhi(xr[ei][ti][0]));
      o[ei][1] = pack2(own[ei][ti][2] + bflo(xr[ei][ti][1]), own[ei][ti][3] + bfhi(xr[ei][ti][1]));
    }
    const u32x4 v = swap16(o[0][0], o[0][1], o[1][0], o[1][1]);
    const int e0 = nt * 128 + w2c * 64 + (ks >> 1) * 32 + (g & 1) * 16 + (g >> 1) * 8;
    *(u32x4*)(out + (size_t)(t_out + ti * 16) * D + e0) = v;
  }
  STAMP(0, 12);
  { const u64 te = __builtin_amdgcn_s_memrealtime() + 2 * (tpb - tpa);
    for (int i = 0; i < 100000 && __builtin_amdgcn_s_memrealtime() < te; ++i) __builtin_amdgcn_s_sleep(1); }
}

extern "C" void kernel_launch(void* const* d_in, const int* in_sizes, int n_in, void* d_out, int out_size,
                              void* d_ws, size_t ws_size, hipStream_t stream) {
  k_fused<<<256, 512, 0, stream>>>((const u16*)d_in[0], (const u16*)d_in[1], (const u16*)d_in[2], (const u16*)d_in[3],
                                   (const u16*)d_in[4], (const u16*)d_in[6], (const u16*)d_in[7], (const u16*)d_in[8],
                                   (char*)d_ws, (u16*)d_out);
}
```

```cpp
#include <hip/hip_runtime.h>
#include <hip/hip_bf16.h>
#include <stdint.h>
#include <utility>

#if defined(__HIP_DEVICE_COMPILE__)
#pragma clang attribute push(__attribute__((target("no-packed-fp32-ops"))), apply_to = function)
#endif
typedef unsigned short u16;
typedef unsigned long long u64;
typedef __attribute__((ext_vector_type(8))) short bf16x8;
typedef __attribute__((ext_vector_type(4))) short s16x4;
typedef __attribute__((ext_vector_type(4))) float f32x4;
typedef __attribute__((ext_vector_type(2))) float f32x2;
typedef __attribute__((ext_vector_type(2))) __bf16 bf2;
typedef __attribute__((ext_vector_type(4))) unsigned u32x4;
typedef __attribute__((ext_vector_type(2))) unsigned u32x2;
typedef __amdgpu_buffer_rsrc_t rsrc_t;
#define LDS_T(T) __attribute__((address_space(3))) T
#define SB() __builtin_amdgcn_sched_barrier(0)
#define DI __device__ __forceinline__
#define MFMA_IP(c, a, b) asm volatile("v_mfma_f32_16x16x32_bf16 %0, %1, %2, %0" : "+v"(c) : "v"(a), "v"(b))
#define MFMA_DRAIN() asm volatile("s_nop 7\n\ts_nop 3" ::: "memory")

constexpr int S = 2048, D = 512, F = 1792;
constexpr unsigned WS_AB = 0;
constexpr unsigned WS_H = WS_AB + 4096 * 512 * 2;
constexpr unsigned WS_P = WS_H + 28 * 2048 * 128;
constexpr unsigned WS_FLAG = WS_P + 64 * 16 * 8192;
constexpr unsigned FLAG_A = WS_FLAG, FLAG_B = WS_FLAG + 256 * 128, FLAG_E = WS_FLAG + 512 * 128;
constexpr unsigned WS_BYTES = FLAG_E + 128;
constexpr int CODE_BYTES = 26624;
constexpr int SLOT = 36864;
constexpr int L_CS1 = 4 * SLOT;
constexpr int L_CS2 = L_CS1 + 4096;
constexpr int L_MF = L_CS2 + 2048;
constexpr int L_VQ = L_MF + 2048;
constexpr int L_ZERO = L_VQ + 512;
constexpr int L_DUMMY = L_ZERO + 256;
constexpr int L_POLL = L_DUMMY + 256;
constexpr int LDS_BYTES = L_POLL + 5120;

template <int N> DI void wait_vm() { asm volatile("s_waitcnt vmcnt(%0)" ::"n"(N) : "memory"); }
DI void wait_lgkm0() { asm volatile("s_waitcnt lgkmcnt(0)" ::: "memory"); }
DI void bar() {
  asm volatile("" ::: "memory");
  __builtin_amdgcn_s_barrier();
  asm volatile("" ::: "memory");
}
DI unsigned pack2(float a, float b) {
  f32x2 p = {a, b};
  return __builtin_bit_cast(unsigned, __builtin_convertvector(p, bf2));
}
DI float bflo(unsigned u) { return __builtin_bit_cast(float, u << 16); }
DI float bfhi(unsigned u) { return __builtin_bit_cast(float, u & 0xffff0000u); }
DI rsrc_t mk_rsrc(const void* p, unsigned bytes) { return __builtin_amdgcn_make_buffer_rsrc((void*)p, 0, bytes, 0x00020000); }
template <int OFF> DI void dma16(rsrc_t r, unsigned lds, unsigned voff, unsigned soff) {
  asm volatile("s_add_u32 m0, %0, %4\n\ts_nop 0\n\tbuffer_load_dwordx4 %1, %2, %3 offen lds" ::"s"(lds), "v"(voff), "s"(r), "s"(soff), "n"(OFF) : "memory", "scc");
}
template <int OFF> DI void dma16c(rsrc_t r, unsigned lds, unsigned voff, unsigned soff) {
  asm volatile("s_add_u32 m0, %0, %4\n\ts_nop 0\n\tbuffer_load_dwordx4 %1, %2, %3 offen sc1 lds" ::"s"(lds), "v"(voff), "s"(r), "s"(soff), "n"(OFF) : "memory", "scc");
}
template <int OFF> DI void dma16_lo32(rsrc_t r, unsigned lds, unsigned voff, unsigned soff) {
  asm volatile("s_add_u32 m0, %0, %4\n\ts_mov_b32 exec_hi, 0\n\tbuffer_load_dwordx4 %1, %2, %3 offen lds\n\ts_mov_b32 exec_hi, -1"
               ::"s"(lds), "v"(voff), "s"(r), "s"(soff), "n"(OFF) : "memory", "scc");
}
DI void touch64(rsrc_t r, unsigned voff, unsigned soff) {
  asm volatile("s_mov_b32 m0, %3\n\ts_nop 0\n\tbuffer_load_dword %0, %1, %2 offen lds" ::"v"(voff), "s"(r), "s"(soff), "n"(L_DUMMY) : "memory");
}
DI void touch32(rsrc_t r, unsigned voff, unsigned soff) {
  asm volatile("s_mov_b32 m0, %3\n\ts_mov_b32 exec_hi, 0\n\tbuffer_load_dword %0, %1, %2 offen lds\n\ts_mov_b32 exec_hi, -1"
               ::"v"(voff), "s"(r), "s"(soff), "n"(L_DUMMY) : "memory");
}
DI void touch8(rsrc_t r, unsigned voff, unsigned soff) {
  asm volatile("s_mov_b32 m0, %3\n\ts_mov_b64 exec, 0xff\n\tbuffer_load_dword %0, %1, %2 offen lds\n\ts_mov_b64 exec, -1"
               ::"v"(voff), "s"(r), "s"(soff), "n"(L_DUMMY) : "memory");
}
DI void st16c(u32x4 v, rsrc_t r, unsigned voff, unsigned soff) {
  asm volatile("buffer_store_dwordx4 %0, %1, %2, %3 offen sc1\n\ts_nop 1" ::"v"(v), "v"(voff), "s"(r), "s"(soff) : "memory");
}
DI unsigned opqs(unsigned v) { asm volatile("" : "+s"(v)); return v; }
DI unsigned opq(unsigned v) { asm volatile("" : "+v"(v)); return v; }
template <class T> DI T ldsr(unsigned a) { return *(const LDS_T(T)*)(size_t)a; }
template <class T> DI void ldsw(unsigned a, T v) { *(LDS_T(T)*)(size_t)a = v; }
DI bf16x8 rd128(unsigned a) { return ldsr<bf16x8>(a); }
DI s16x4 rdtr1(unsigned a) { return __builtin_amdgcn_ds_read_tr16_b64_v4i16((LDS_T(s16x4)*)(size_t)a); }
DI u32x4 swap16(unsigned a0, unsigned a1, unsigned b0, unsigned b1) {
  auto s0 = __builtin_amdgcn_permlane16_swap(a0, b0, false, false);
  auto s1 = __builtin_amdgcn_permlane16_swap(a1, b1, false, false);
  return u32x4{s0[0], s1[0], s0[1], s1[1]};
}
DI unsigned umx(unsigned a, unsigned b) { return a > b ? a : b; }
DI bool all64(bool p) { return __builtin_amdgcn_ballot_w64(!p) == 0; }
DI u64 ld_flag(const char* p) { return __hip_atomic_load((const u64*)p, __ATOMIC_RELAXED, __HIP_MEMORY_SCOPE_AGENT); }
DI void st_flag(char* p, u64 v) { __hip_atomic_store((u64*)p, v, __ATOMIC_RELAXED, __HIP_MEMORY_SCOPE_AGENT); }
DI void spin_flags(const char* p, u64 tag) {
#pragma unroll 1
  for (unsigned spin = 0; spin < (1u << 20); ++spin) {
    if (all64(ld_flag(p) == tag)) break;
    __builtin_amdgcn_s_sleep(1);
  }
}

#define STAMP(k, i)
#define STAMPF(i)

template <int N> struct IC { static constexpr int value = N; };
template <class Fn, int... Is> DI void unroll_impl(Fn&& f, std::integer_sequence<int, Is...>) { (f(IC<Is>{}), ...); }
template <int N, class Fn> DI void unroll(Fn&& f) { unroll_impl(f, std::make_integer_sequence<int, N>{}); }
#define CV(x) decltype(x)::value

constexpr int st_ops(int stage) { return stage < 8 ? 5 : 4; }
#define X0H 3
#define PF1M 0x55
constexpr int PF1(int p) { return p >= 0 && p < 7 ? (PF1M >> p) & 1 : 0; }
constexpr int PFI(int p) { return __builtin_popcount(PF1M & ((1 << p) - 1)); }
#define PREFILL 0
constexpr int PRE0 = PREFILL == 0 ? 3 : 0;
constexpr int allowM(int p) { return p == 0 ? 5 + PRE0 : PF1(p - 2) + st_ops(p + 2) + PF1(p - 1); }

__global__ __launch_bounds__(512, 2) void k_fused(const u16* __restrict__ x, const u16* __restrict__ cre,
                                                  const u16* __restrict__ cim, const u16* __restrict__ wre,
                                                  const u16* __restrict__ wim, const u16* __restrict__ wg,
                                                  const u16* __restrict__ wu, const u16* __restrict__ wd,
                                                  char* __restrict__ ws, u16* __restrict__ out) {
  __shared__ __attribute__((aligned(1024))) char shm[LDS_BYTES];
  asm volatile("" ::"v"((LDS_T(char)*)shm));
  int lane = threadIdx.x & 63;
  const int wid = __builtin_amdgcn_readfirstlane(threadIdx.x >> 6);
  const int id = blockIdx.x, xcd = id & 7, idx = id >> 3;
  const int c = xcd >> 1, cgl = idx & 3;
  const int rb = (xcd & 1) * 8 + (idx >> 2);
  const bool isQ = cgl == 3;
  const int unit1 = 7 * c + 4 + cgl, unit2 = 7 * c + cgl;
  const int rot = ((idx >> 2) + 2 * cgl) & 7;
  int r = lane & 15, g = lane >> 4;
#define RELANE() do { lane = opq(__builtin_amdgcn_mbcnt_hi(~0u, __builtin_amdgcn_mbcnt_lo(~0u, 0u))); r = lane & 15; g = lane >> 4; } while (0)
#define sw ((r >> 1) & 7)
  const int wr = wid & 3, gq = wid >> 2;
  if (wid == 7) {
    const u64 pc = __builtin_amdgcn_s_getpc() & ~(u64)127;
    touch64(mk_rsrc((const void*)pc, CODE_BYTES), lane * 512, cgl * 128);
  }
  STAMP(0, 0);
#define FS(k)
#define FDUMP()

  u64 tpa, tpb;
  {
    const rsrc_t rp = mk_rsrc((ws + ((size_t)64 << 20) + (size_t)id * 65536), 65536);
    tpa = __builtin_amdgcn_s_memrealtime();
    __builtin_amdgcn_s_waitcnt(0xC07F);
#pragma unroll
    for (int i = 0; i < 8; ++i)
      st16c(u32x4{1u, 2u, 3u, 4u}, rp, lane * 16, wid * 8192 + i * 1024);
    wait_vm<0>();
    bar();
    tpb = __builtin_amdgcn_s_memrealtime();
    __builtin_amdgcn_s_waitcnt(0xC07F);
  }
  const rsrc_t rx = mk_rsrc(x + rb * 128 * D, 128 * D * 2);
  const rsrc_t rre = mk_rsrc(wre + (c * 4 + cgl) * 32 * D, 32 * D * 2);
  const u16* wgu = gq ? wu : wg;
  const rsrc_t r1 = isQ ? mk_rsrc(wim + c * 128 * D, 128 * D * 2) : mk_rsrc(wgu + unit1 * 64, D * F * 2 - unit1 * 128);
  const rsrc_t r2 = mk_rsrc(wgu + unit2 * 64, D * F * 2 - unit2 * 128);
  const rsrc_t rc = mk_rsrc(((c & 2) ? cim : cre) + rb * 128 * S + (c & 1) * 1024, 128 * S * 2 - (c & 1) * 2048);
  const rsrc_t rws = mk_rsrc(ws, WS_BYTES);
  unsigned vrow[2], vw[2], v1[2];
#pragma unroll
  for (int i = 0; i < 2; ++i) {
    const int row = wid * 16 + i * 8 + (lane >> 3);
    vrow[i] = row * 1024 + (((lane & 7) ^ ((row >> 1) & 7)) << 4);
    const int kr = wr * 16 + i * 8 + (lane >> 3);
    const int f = ((kr >> 1) & 1) | (((kr >> 3) & 1) << 1);
    vw[i] = kr * (F * 2) + (((lane & 7) ^ (f << 1)) << 4);
    v1[i] = isQ ? vrow[i] : vw[i];
  }
  const unsigned vd32 = (wid * 4 + (lane >> 3)) * 1024 + (((lane & 7) ^ (((wid * 4 + (lane >> 3)) >> 1) & 7)) << 4);
  const unsigned step1 = isQ ? 128 : 64 * F * 2;
  const unsigned lw2 = wid * 2048, lw5 = wid * 512;
  auto issueA = [&](auto sc, auto qc) {
    constexpr int stage = CV(sc), q = CV(qc), sl = (stage & 3) * SLOT;
    if constexpr (stage < 8) {
      const int kp = (stage + rot) & 7;
      if constexpr (q < 2) dma16<sl + q * 1024>(rx, lw2, vrow[q], kp * 128);
      else if constexpr (q < 4) dma16<sl + 16384 + (q - 2) * 1024>(r1, lw2, v1[q - 2], kp * step1);
      else dma16_lo32<sl + 32768>(rre, lw5, vd32, kp * 128);
    } else if constexpr (q < 4) {
      const int kp = ((stage - 8) * 2 + (q >> 1) + rot) & 7;
      dma16<sl + (q >> 1) * 16384 + (q & 1) * 1024>(r2, lw2, vw[q & 1], kp * (64 * F * 2));
    }
  };
  unroll<5>([&](auto q) { issueA(IC<0>{}, q); });
  unroll<5>([&](auto q) { issueA(IC<1>{}, q); });
#pragma unroll
  for (int i = 0; i < 3; ++i)
    st16c(u32x4{~0u, ~0u, ~0u, ~0u}, rws, lane * 16, WS_P + ((rb * 4 + cgl) * 16 + c * 4 + ((c + 1 + i) & 3)) * 8192 + wid * 1024);
  u64 tag = *(const __attribute__((address_space(4))) u64*)(ws + FLAG_E) + 1;
  asm volatile("" : "+s"(tag));
  ldsw<u32x4>(L_ZERO, u32x4{0u, 0u, 0u, 0u});
  const unsigned vpf1 = ((lane >> 2) & 1) * (S * 2) + (lane & 3) * 128;

  const int jl = wr >> 1, bl = wr & 1;
  const unsigned xa = r * 128 + (((4 * jl + g) ^ sw) << 4);
  unsigned wa;
  {
    const int q = (lane & 15) >> 2, p = lane & 3;
    const int f = (q >> 1) | ((g & 1) << 1);
    wa = (8 * g + q) * 128 + (((2 * gq + bl) ^ f) << 5) + p * 8 + jl * 4096;
  }
  bf16x8 xf[8][2][2];
  s16x4 wh[4][2][2];
  bf16x8 df[2][2];
#define WF(f, j) __builtin_shufflevector(wh[f][j][0], wh[f][j][1], 0, 1, 2, 3, 4, 5, 6, 7)
  f32x4 acc[4][2], a0[2], ssum, ssq, cs0, cs1, csd;
  bf16x8 ones = {0x3f80, 0x3f80, 0x3f80, 0x3f80, 0x3f80, 0x3f80, 0x3f80, 0x3f80};
  const f32x4 z4 = {0.f, 0.f, 0.f, 0.f};
  unsigned xA[2], dA[2], wo[2];
#pragma unroll
  for (int b = 0; b < 2; ++b) { xA[b] = xa + (wr * 32 + (b ^ gq) * 16) * 128; dA[b] = xa + 32768 + (b ^ bl) * 2048; }
  auto base_w_tr = [&](unsigned st, int j) {
    const unsigned so = opqs(st);
    wo[0] = (wa ^ (j << 12)) + so;
    wo[1] = (wa ^ 32 ^ (j << 12)) + so;
  };
  auto read_w_tr1 = [&](int k, int j) {
    wh[k >> 1][j][k & 1] = rdtr1(wo[(k >> 1) & 1] + (k >> 2) * 8192 + (k & 1) * 512);
  };
  auto base_w_q = [&](unsigned st, int j) {
    const unsigned so = opqs(st) + gq * 8192 - 16384;
    wo[0] = (dA[0] ^ (j << 6)) + so;
    wo[1] = (dA[1] ^ (j << 6)) + so;
  };
  auto read_w_q1 = [&](int f, int j) {
    const bf16x8 v = rd128(wo[f & 1] + (f >> 1) * 4096);
    wh[f][j][0] = __builtin_shufflevector(v, v, 0, 1, 2, 3);
    wh[f][j][1] = __builtin_shufflevector(v, v, 4, 5, 6, 7);
  };
  unsigned xso;
  auto rd1 = [&](auto qc, int p, int j, int k) {
    constexpr bool Q = CV(qc);
    if (k == 0) { if (Q) base_w_q((p & 3) * SLOT, j); else base_w_tr((p & 3) * SLOT + 16384, j); xso = opqs((p & 3) * SLOT); }
    if (k == 2 || k == 3) xf[p][k - 2][j] = rd128((xA[k - 2] ^ (j << 6)) + xso);
    else if (k >= 10) df[k - 10][j] = rd128((dA[k - 10] ^ (j << 6)) + xso);
    else {
      const int kk = k < 2 ? k : k - 2;
      if (!Q) read_w_tr1(kk, j);
      else if ((kk & 1) == 0) read_w_q1(kk >> 1, j);
    }
  };
  auto period_op = [&](auto pc, auto kc) {
    constexpr int p = CV(pc), k = CV(kc);
    if constexpr (k < st_ops(p + 3)) issueA(IC<p + 3>{}, kc);
    else if constexpr (k == st_ops(p + 3) && PF1(p))
      touch8(rc, vpf1, (cgl * 32 + wid * 4 + (PFI(p) & 1) * 2) * (S * 2) + (PFI(p) >> 1) * 512);
  };
  auto pass1 = [&](auto qc) {
#pragma unroll
    for (int a = 0; a < 4; ++a) { acc[a][0] = z4; acc[a][1] = z4; }
    a0[0] = z4; a0[1] = z4; ssum = z4; ssq = z4; cs0 = z4; cs1 = z4; csd = z4;
    asm volatile("" : "+v"(acc[0][0]), "+v"(acc[0][1]), "+v"(acc[1][0]), "+v"(acc[1][1]), "+v"(acc[2][0]), "+v"(acc[2][1]), "+v"(acc[3][0]), "+v"(acc[3][1]), "+v"(a0[0]), "+v"(a0[1]));
    asm volatile("" : "+v"(ssum), "+v"(ssq), "+v"(cs0), "+v"(cs1), "+v"(csd), "+v"(ones));
    wait_vm<5 + PRE0>();
    bar();
    STAMP(0, 1);
#pragma unroll
    for (int k = 0; k < 12; ++k) rd1(qc, 0, 0, k);
    SB();
    unroll<16>([&](auto hc) {
      constexpr int h = CV(hc), p = h >> 1, j = h & 1;
      if constexpr (j == 1) { wait_vm<allowM(p)>(); bar(); }
      SB();
      unroll<14 - j>([&](auto nc) {
        constexpr int n = CV(nc);
        if constexpr (n < 8) MFMA_IP(acc[n >> 1][n & 1], WF(n >> 1, j), xf[p][n & 1][j]);
        else if constexpr (n == 8) MFMA_IP(ssum, ones, xf[p][0][j]);
        else if constexpr (n == 9) MFMA_IP(ssq, xf[p][0][j], xf[p][0][j]);
        else if constexpr (n == 10) { if constexpr (j == 0) MFMA_IP(cs0, ones, WF(0, 0)); else MFMA_IP(cs1, ones, WF(2, 1)); }
        else if constexpr (n < 13) MFMA_IP(a0[n - 11], df[n - 11][j], xf[p][0][j]);
        else MFMA_IP(csd, ones, df[0][0]);
        SB();
        constexpr int pn = (h + 1) >> 1, jn = (h + 1) & 1;
        if constexpr (h < 15) { if constexpr (n < 12) rd1(qc, pn, jn, n); }
        else { if constexpr (n == 0) base_w_tr(0, 0); if constexpr (n < 8) read_w_tr1(n, 0); }
        if constexpr (h == 0) { if constexpr (n % 3 == 1) issueA(IC<2>{}, IC<n / 3>{}); }
        else if constexpr (h == 15) { if constexpr (n % 3 == 2) period_op(IC<7>{}, IC<n / 3>{}); }
        else if constexpr (n == 2 || n == 6 || n == 10) {
          if constexpr (j == 1) period_op(IC<p>{}, IC<(n - 2) / 4>{});
          else period_op(IC<p - 1>{}, IC<3 + (n - 2) / 4>{});
        }
        SB();
      });
    });
  };
  if (isQ) pass1(IC<1>{}); else pass1(IC<0>{});
  FS(0);

  auto put_cs = [&](float v, unsigned addr) {
    const unsigned d1 = pack2(v, 0.f);
    ldsw<u32x2>(addr, u32x2{pack2(v, v - bflo(d1)), d1});
  };
  RELANE();
  {
    const unsigned row = wr * 32 + gq * 16 + r;
    asm volatile("s_nop 7\n\ts_nop 3" : "+v"(cs0), "+v"(cs1), "+v"(csd), "+v"(ssum), "+v"(ssq));
    ldsw<f32x4>(gq * SLOT + 32768 + wr * 1024 + lane * 16, ssq);
    const float c01 = g == 0 ? cs0[0] : cs1[0];
    put_cs(g < 2 ? c01 : csd[0], L_CS1 + gq * 2048 + bl * 256 + jl * 8 + g * 512 + r * 16);
    const float nm = ssum[0] * (-1.0f / D);
    const unsigned d0 = pack2(nm, nm), d1 = pack2(nm - bflo(d0), 0.f);
    ldsw<u32x4>(L_MF + row * 16, u32x4{d0, d1, d0, d1});
    ldsw<float>(L_VQ + row * 4, fmaf(-nm, nm, 1e-5f));
  }
  wait_lgkm0();
  FS(1);
  wait_vm<PF1(6) + 4>();
  bar();
  FS(2);
  STAMP(0, 2);

  float var[2];
  bf16x8 mf[2], cf[4], cd[2];
  auto corr_rd = [&](f32x4 (&ac_)[4][2], unsigned csbase) {
    asm volatile("" : "+v"(ac_[0][0]), "+v"(ac_[0][1]), "+v"(ac_[1][0]), "+v"(ac_[1][1]), "+v"(ac_[2][0]), "+v"(ac_[2][1]), "+v"(ac_[3][0]), "+v"(ac_[3][1]));
    const unsigned r16 = r * 16;
#pragma unroll
    for (int b = 0; b < 2; ++b) mf[b] = rd128(g == 0 ? L_MF + (wr * 32 + (b ^ gq) * 16) * 16 + r16 : L_ZERO);
    const unsigned ca0 = csbase + bl * 256 + r16, ca1 = csbase + (bl ^ 1) * 256 + r16;
    cf[0] = rd128(ca0); cf[1] = rd128(ca1); cf[2] = rd128(ca0 + 512); cf[3] = rd128(ca1 + 512);
    return u32x2{ca0, ca1};
  };
  auto corr_mm = [&](f32x4 (&ac_)[4][2]) {
    SB();
#pragma unroll
    for (int b = 0; b < 2; ++b)
#pragma unroll
      for (int f = 0; f < 4; ++f) MFMA_IP(ac_[f][b], cf[f], mf[b]);
    MFMA_DRAIN();
    SB();
  };
  float K1, K2;
  auto ffn_k = [&](int b) {
    const float rs = __builtin_amdgcn_rsqf(var[b]);
    K1 = -2.302208198f * rs; K2 = -0.1029432397f * rs * rs * rs;
  };
  auto ffn_pair = [&](const f32x4 (&ac_)[4][2], int b, int k) {
    float h[2];
#pragma unroll
    for (int q = 0; q < 2; ++q) {
      const float a = ac_[k >> 1][b][(k & 1) * 2 + q];
      const float e = __builtin_amdgcn_exp2f(a * fmaf(a * a, K2, K1));
      h[q] = a * ac_[2 + (k >> 1)][b][(k & 1) * 2 + q] * __builtin_amdgcn_rcpf(fmaf(e, var[b], var[b]));
    }
    return pack2(h[0], h[1]);
  };
  auto ffn_calc = [&](const f32x4 (&ac_)[4][2], int b) {
    ffn_k(b);
    const unsigned o0 = ffn_pair(ac_, b, 0), o1 = ffn_pair(ac_, b, 1), o2 = ffn_pair(ac_, b, 2), o3 = ffn_pair(ac_, b, 3);
    return swap16(o0, o1, o2, o3);
  };
#define VST (r * 64 + (((g & 1) ^ bl) * 2 + (g >> 1)) * 16)
  auto ffn_store = [&](u32x4 v, int unit, int b) {
    st16c(v, rws, VST, WS_H + ((unit * 2 + gq) * S + rb * 128 + wr * 32 + (b ^ gq) * 16) * 64);
  };
  auto dft_store = [&](f32x4 v0, f32x4 v1_, float rs, int grow, int eblk) {
    const u32x4 v = swap16(pack2(v0[0] * rs, v0[1] * rs), pack2(v0[2] * rs, v0[3] * rs), pack2(v1_[0] * rs, v1_[1] * rs), pack2(v1_[2] * rs, v1_[3] * rs));
    st16c(v, rws, VST, WS_AB + (eblk * 4096 + grow) * 64);
  };
  {
    const u32x2 ca = corr_rd(acc, L_CS1 + gq * 2048);
    cd[0] = rd128(ca[0] + 1024); cd[1] = rd128(ca[1] + 1024);
    const unsigned dg = (r >> 2) * 256 + r * 16 + (r & 3) * 4;
#pragma unroll
    for (int b = 0; b < 2; ++b)
      var[b] = fmaxf(fmaf(ldsr<float>((b ^ gq) * SLOT + 32768 + wr * 1024 + dg), 1.0f / D, ldsr<float>(L_VQ + (wr * 32 + (b ^ gq) * 16 + r) * 4)), 1e-5f);
    SB();
    unroll<4>([&](auto q) { issueA(IC<11>{}, q); });
    SB();
    MFMA_IP(a0[0], cd[0], mf[0]); MFMA_IP(a0[1], cd[1], mf[0]);
    corr_mm(acc);
  }
  FS(3);
  dft_store(a0[0], a0[1], __builtin_amdgcn_rsqf(var[0]), rb * 128 + wr * 32 + gq * 16, c * 4 + cgl);
  if (isQ) {
#pragma unroll
    for (int b = 0; b < 2; ++b) {
      const float rs = -__builtin_amdgcn_rsqf(var[b]);
#pragma unroll
      for (int h = 0; h < 2; ++h)
        dft_store(acc[2 * h][b], acc[2 * h + 1][b], rs, S + rb * 128 + wr * 32 + (b ^ gq) * 16, c * 4 + gq * 2 + h);
    }
  }
  FS(4);
  const int ks = c, mt = rb, nt = cgl, tile = mt * 4 + nt;
  f32x4 ac2[4][2];
  auto half2 = [&](auto hc) {
    constexpr int h = CV(hc), p = h >> 1, j = h & 1;
    unroll<9>([&](auto nc) {
      constexpr int n = CV(nc);
      if constexpr (n < 8) MFMA_IP(ac2[n >> 1][n & 1], WF(n >> 1, j), xf[p][n & 1][j]);
      else if constexpr (j == 0) MFMA_IP(cs0, ones, WF(0, 0));
      else MFMA_IP(cs1, ones, WF(2, 1));
      SB();
      constexpr int pn = (h + 1) >> 1, jn = (h + 1) & 1;
      if constexpr (h < 15) {
        if constexpr (n == 0) base_w_tr(((8 + (pn >> 1)) & 3) * SLOT + (pn & 1) * 16384, jn);
        if constexpr (n < 8) read_w_tr1(n, jn);
      }
      SB();
    });
  };
  unsigned va[4], vb[4];
  auto init_vab = [&](auto hc) {
    const int r0 = wr * 32 + (lane >> 3), k0 = wr * 16 + (lane >> 4);
#pragma unroll
    for (int i = 0; i < 4; ++i) {
      const int row = r0 + i * 8, kr = k0 + i * 4;
      const int ca = (lane & 7) ^ ((row >> 1) & 7), cb = (lane & 15) ^ (((kr & 3) | (((kr >> 3) & 1) << 2)) << 1);
      va[i] = CV(hc) ? (ca >> 2) * (S * 64) + row * 64 + (ca & 3) * 16 : row * (S * 2) + ca * 16;
      vb[i] = CV(hc) ? kr * 1024 + cb * 16 : (cb >> 2) * (4096 * 64) + kr * 64 + (cb & 3) * 16;
    }
  };
  auto issueC1 = [&](int step, auto qc) {
    constexpr int q = CV(qc);
    const unsigned dst = (step & 3) * SLOT + wr * 4096;
    if constexpr (q < 4) dma16<q * 1024>(rc, dst, va[q], step * 128);
    else dma16c<16384 + (q - 4) * 1024>(rws, dst, vb[q - 4], WS_AB + (nt * 4 * 4096 + ks * 1024 + step * 64) * 64);
  };
  auto issueC = [&](int step) { unroll<8>([&](auto q) { issueC1(step, q); }); };
  auto touchC = [&](int quarter) {
    touch32(rc, (lane >> 2) * (S * 2) + (lane & 3) * 128, (nt * 32 + wr * 8) * (S * 2) + quarter * 512);
  };

  if (gq == 0) {
    u32x4 hv;
    if (!isQ) hv = ffn_calc(acc, 0);
    FS(5);
    wait_vm<0>();
    FS(6);
    bar();
    FS(7);
    if (wid == 0 && lane == 0) st_flag(ws + FLAG_A + id * 128, tag);
    touchC(2);
    if (!isQ) ffn_store(hv, unit1, 0);
    if (!isQ) ffn_store(ffn_calc(acc, 1), unit1, 1);
  }
  STAMP(0, 3);
  FS(8);
#pragma unroll
  for (int a = 0; a < 4; ++a) { ac2[a][0] = z4; ac2[a][1] = z4; }
  cs0 = z4; cs1 = z4;
  asm volatile("s_nop 1" : "+v"(ac2[0][0]), "+v"(ac2[0][1]), "+v"(ac2[1][0]), "+v"(ac2[1][1]), "+v"(ac2[2][0]), "+v"(ac2[2][1]), "+v"(ac2[3][0]), "+v"(ac2[3][1]), "+v"(cs0), "+v"(cs1));
  SB();
  unroll<X0H>([&](auto hc) { half2(hc); });
  FS(9);
  if (gq == 1) { wait_vm<0>(); FS(10); bar(); }
  FS(11);
  unroll<8 - X0H>([&](auto hc) { half2(IC<X0H + CV(hc)>{}); });
  FS(12);
  if (gq == 1) {
    RELANE();
    if (!isQ) { ffn_store(ffn_calc(acc, 0), unit1, 0); ffn_store(ffn_calc(acc, 1), unit1, 1); }
    init_vab(IC<0>{});
    FS(15);
    bar();
    FS(16);
    dma16c<L_POLL>(rws, wr * 1024, (lane & 31) * 1024, FLAG_A + (2 * cgl + (c & 1)) * 128);
    unroll<4>([&](auto q) { issueC1(0, q); });
    unroll<4>([&](auto q) { issueC1(1, q); });
    wait_vm<8>();
    FS(17);
    if (!all64(ldsr<u64>(L_POLL + wr * 1024 + lane * 16) == tag)) spin_flags(ws + FLAG_A + ((2 * cgl + (c & 1)) + 8 * (lane & 31)) * 128, tag);
    FS(13);
    unroll<4>([&](auto q) { issueC1(0, IC<4 + CV(q)>{}); });
    unroll<4>([&](auto q) { issueC1(1, IC<4 + CV(q)>{}); });
    FS(18);
    bar();
  }
  FS(19);
  SB();
  half2(IC<8>{});
  FS(20);
  if (gq == 0) { bar(); RELANE(); touchC(3); }
  FS(21);
  unroll<7>([&](auto hc) { half2(IC<9 + CV(hc)>{}); });
  FS(24);
  RELANE();
  asm volatile("s_nop 7\n\ts_nop 3" : "+v"(cs0), "+v"(cs1));
  put_cs((g & 1) ? cs1[0] : cs0[0], L_CS2 + gq * 1024 + bl * 256 + jl * 8 + (g & 1) * 512 + r * 16);
  wait_lgkm0();
  FS(25);

  if (gq == 1) {
    init_vab(IC<0>{});
    wait_vm<0>();
    FS(26);
    bar();
    FS(27);
    corr_rd(ac2, L_CS2 + 1024);
    unroll<4>([&](auto q) { issueC1(2, q); });
    FS(28);
    bar();
    FS(29);
    unroll<4>([&](auto q) { issueC1(2, IC<4 + CV(q)>{}); });
    corr_mm(ac2);
    unsigned o[4];
    auto slice = [&](auto kc) {
      constexpr int k = CV(kc), b = k / 6, t = k % 6;
      if constexpr (b < 2) {
        if constexpr (t == 0) ffn_k(b);
        else if constexpr (t < 5) o[t - 1] = opq(ffn_pair(ac2, b, t - 1));
        else ffn_store(swap16(o[0], o[1], o[2], o[3]), unit2, b);
      }
      SB();
    };
    unroll<8>([&](auto q) { issueC1(3, q); SB(); if constexpr (CV(q) & 1) slice(IC<CV(q) / 2>{}); });
    wait_vm<8>();
    FS(30);
    bar();
    FS(31);
    unroll<8>([&](auto q) { issueC1(4, q); SB(); if constexpr (CV(q) & 1) slice(IC<4 + CV(q) / 2>{}); });
    wait_vm<9>();
    FS(32);
    bar();
    FS(33);
    unroll<8>([&](auto q) { issueC1(5, q); SB(); if constexpr (CV(q) & 1) slice(IC<8 + CV(q) / 2>{}); });
    wait_vm<9>();
    FS(34);
    bar();
    issueC(6);
    wait_vm<8>();
    bar();
    FS(35);
    wait_vm<0>();
    FDUMP();
    issueC(7);
    const unsigned fbo = FLAG_B + ((lane & 4) ? (id & ~6) + 2 * (lane & 3) : xcd + 8 * ((idx & ~3) + (lane & 3))) * 128;
#pragma unroll 1
    for (int i = 5; i < 13; ++i) {
      wait_vm<8>();
      if (i == 12) {
        if (wr == 0 && !all64(ldsr<u64>(L_POLL + 4096 + lane * 16) == tag)) spin_flags(ws + fbo, tag);
        if (wr == 1 && id == 0) {
#pragma unroll 1
          for (int q = 0; q < 4; ++q)
            if (!all64(ldsr<u64>(L_CS1 + q * 1024 + lane * 16) == tag)) spin_flags(ws + FLAG_A + (q * 64 + lane) * 128, tag);
          if (lane == 0) st_flag(ws + FLAG_E, tag);
        }
      }
      bar();
      if (i == 5 && wr == 2 && lane == 0) st_flag(ws + FLAG_B + id * 128, tag);
      issueC(i + 3);
      if (i == 10) {
        if (wr == 0) dma16c<L_POLL + 4096>(rws, 0, fbo, 0);
        if (wr == 1 && id == 0) unroll<4>([&](auto q) { dma16c<L_CS1 + CV(q) * 1024>(rws, 0, lane * 128, FLAG_A + CV(q) * 8192); });
      }
    }
    init_vab(IC<1>{});
    const rsrc_t rd = mk_rsrc(wd, F * D * 2);
#pragma unroll 1
    for (int i = 13; i < 20; ++i) {
      wait_vm<8>();
      bar();
      const unsigned dst = ((i + 3) & 3) * SLOT + wr * 4096;
      const unsigned so = WS_H + ((ks * 7 + i - 13) * 2 * S + mt * 128) * 64, sb = (ks * 7 + i - 13) * 65536 + nt * 256;
      unroll<4>([&](auto q) { dma16c<CV(q) * 1024>(rws, dst, va[CV(q)], so); });
      unroll<4>([&](auto q) { dma16<16384 + CV(q) * 1024>(rd, dst, vb[CV(q)], sb); });
    }
    wait_vm<8>();
    bar();
    wait_vm<0>();
    bar();
    return;
  }

  bar();
  FS(26);
  STAMP(0, 4);
  corr_rd(ac2, L_CS2);
  touch32(mk_rsrc(wd, F * D * 2), (lane < 28 ? lane : 27) * 128, (ks * 448 + idx * 14) * 1024 + wr * 3584);
  corr_mm(ac2);
  FS(27);
  ffn_store(ffn_calc(ac2, 0), unit2, 0);
  FS(28);
  ffn_store(ffn_calc(ac2, 1), unit2, 1);
  FS(31);
  STAMP(0, 5);

  const int w2r = wr >> 1, w2c = wr & 1;
  f32x4 bc[4][4];
#pragma unroll
  for (int a = 0; a < 4; ++a)
#pragma unroll
    for (int b = 0; b < 4; ++b) bc[a][b] = z4;
  unsigned ac[2], bd[4];
#pragma unroll
  for (int j = 0; j < 2; ++j) ac[j] = (w2r * 64 + r) * 128 + (((4 * j + g) ^ sw) << 4);
  {
    const int q = (lane & 15) >> 2, p = lane & 3;
    const int f = q | ((g & 1) << 2);
#pragma unroll
    for (int e = 0; e < 4; ++e) bd[e] = 16384 + (8 * g + q) * 256 + (((w2c * 4 + e) ^ f) << 5) + p * 8;
  }
  bf16x8 fa[2][4];
  s16x4 fb[2][4][2];
  int dlt = 2 * SLOT;
  auto sub = [&](auto mbc, auto spc, auto jc, auto rdc, auto tgc) {
    constexpr int mb = CV(mbc), nb = mb ^ 1, j = CV(jc);
    constexpr unsigned so = CV(spc) * SLOT;
    unroll<16>([&](auto nc) {
      constexpr int n = CV(nc), ei = n >> 2, ti = n & 3;
      MFMA_IP(bc[ei][ti], __builtin_shufflevector(fb[mb][ei][0], fb[mb][ei][1], 0, 1, 2, 3, 4, 5, 6, 7), fa[mb][ti]);
      SB();
      if constexpr (CV(rdc)) {
        if constexpr (n == 0) fa[nb][0] = rd128(ac[j] + so);
        if constexpr (n == 1 || n == 2) fb[nb][0][n - 1] = rdtr1(bd[0] + so + j * 8192 + (n - 1) * 1024);
        if constexpr (n == 3) fa[nb][1] = rd128(ac[j] + so + 2048);
        if constexpr (n == 5) fa[nb][2] = rd128(ac[j] + so + 4096);
        if constexpr (n == 7) fa[nb][3] = rd128(ac[j] + so + 6144);
        if constexpr (n >= 8 && n < 14) fb[nb][(n - 6) >> 1][n & 1] = rdtr1(bd[(n - 6) >> 1] + so + j * 8192 + (n & 1) * 1024);
      }
      if constexpr (CV(tgc)) {
        if constexpr (n == 4) { ac[0] = opq(ac[0] + dlt); }
        if constexpr (n == 6) { bd[0] = opq(bd[0] + dlt); }
        if constexpr (n == 14) { ac[1] = opq(ac[1] + dlt); bd[1] = opq(bd[1] + dlt); }
        if constexpr (n == 15) { bd[2] = opq(bd[2] + dlt); bd[3] = opq(bd[3] + dlt); dlt = -dlt; }
      }
      SB();
    });
  };
#pragma unroll
  for (int a = 0; a < 4; ++a) asm volatile("" : "+v"(bc[a][0]), "+v"(bc[a][1]), "+v"(bc[a][2]), "+v"(bc[a][3]));
  FS(32);
  bar();
  FS(33);
  FDUMP();
  STAMP(0, 6);
#pragma unroll
  for (int i = 0; i < 4; ++i) {
    fa[0][i] = rd128(ac[0] + i * 2048);
    fb[0][i][0] = rdtr1(bd[i]);
    fb[0][i][1] = rdtr1(bd[i] + 1024);
  }
  SB();
#pragma unroll 1
  for (int i = 0; i < 22; i += 2) {
    sub(IC<0>{}, IC<0>{}, IC<1>{}, IC<1>{}, IC<0>{});
    bar();
    sub(IC<1>{}, IC<1>{}, IC<0>{}, IC<1>{}, IC<0>{});
    sub(IC<0>{}, IC<1>{}, IC<1>{}, IC<1>{}, IC<1>{});
    wait_vm<0>();
    bar();
    sub(IC<1>{}, IC<0>{}, IC<0>{}, IC<1>{}, IC<0>{});
  }
  RELANE();
  const int t_out = mt * 128 + w2r * 64 + (ks & 1) * 32 + r;
  const int e_out = nt * 128 + w2c * 64 + (ks >> 1) * 32 + g * 4;
  u32x2 xr[2][2];
#pragma unroll
  for (int ei = 0; ei < 2; ++ei)
#pragma unroll
    for (int ti = 0; ti < 2; ++ti) xr[ei][ti] = *(const u32x2*)(x + (size_t)(t_out + ti * 16) * D + e_out + ei * 16);
  SB();
  sub(IC<0>{}, IC<0>{}, IC<1>{}, IC<1>{}, IC<0>{});
  sub(IC<1>{}, IC<0>{}, IC<0>{}, IC<0>{}, IC<0>{});
  MFMA_DRAIN();
  SB();
  STAMP(0, 9);

  f32x4 own[2][2];
#pragma unroll
  for (int q = 0; q < 4; ++q) {
    const int e2 = (q >> 1) * 2, t2 = (q & 1) * 2;
    if (q == ks) {
#pragma unroll
      for (int ei = 0; ei < 2; ++ei)
#pragma unroll
        for (int ti = 0; ti < 2; ++ti) own[ei][ti] = bc[e2 + ei][t2 + ti];
    } else {
#pragma unroll
      for (int ei = 0; ei < 2; ++ei) {
        const f32x4 a = bc[e2 + ei][t2], b = bc[e2 + ei][t2 + 1];
        st16c(u32x4{pack2(a[0], a[1]), pack2(a[2], a[3]), pack2(b[0], b[1]), pack2(b[2], b[3])}, rws, lane * 16,
              WS_P + (tile * 16 + q * 4 + ks) * 8192 + wr * 2048 + ei * 1024);
      }
    }
  }
  STAMP(0, 10);
  u32x4 pv[6];
#pragma unroll 1
  for (unsigned spin = 0; spin < (1u << 20); ++spin) {
    unsigned mx = 0;
#pragma unroll
    for (int i = 0; i < 6; ++i)
      pv[i] = __builtin_amdgcn_raw_buffer_load_b128(rws, lane * 16, WS_P + (tile * 16 + ks * 4 + ((ks + 1 + (i >> 1)) & 3)) * 8192 + wr * 2048 + (i & 1) * 1024, 16);
#pragma unroll
    for (int i = 0; i < 6; ++i) mx = umx(umx(mx, umx(pv[i][0], pv[i][1])), umx(pv[i][2], pv[i][3]));
    if (all64(mx != ~0u)) break;
    asm volatile("" ::: "memory");
  }
  STAMP(0, 11);
#pragma unroll
  for (int i = 0; i < 3; ++i)
#pragma unroll
    for (int ei = 0; ei < 2; ++ei)
#pragma unroll
      for (int ti = 0; ti < 2; ++ti) {
        own[ei][ti][0] += bflo(pv[2 * i + ei][2 * ti]);
        own[ei][ti][1] += bfhi(pv[2 * i + ei][2 * ti]);
        own[ei][ti][2] += bflo(pv[2 * i + ei][2 * ti + 1]);
        own[ei][ti][3] += bfhi(pv[2 * i + ei][2 * ti + 1]);
      }
#pragma unroll
  for (int ti = 0; ti < 2; ++ti) {
    unsigned o[2][2];
#pragma unroll
    for (int ei = 0; ei < 2; ++ei) {
      o[ei][0] = pack2(own[ei][ti][0] + bflo(xr[ei][ti][0]), own[ei][ti][1] + bfhi(xr[ei][ti][0]));
      o[ei][1] = pack2(own[ei][ti][2] + bflo(xr[ei][ti][1]), own[ei][ti][3] + bfhi(xr[ei][ti][1]));
    }
    const u32x4 v = swap16(o[0][0], o[0][1], o[1][0], o[1][1]);
    const int e0 = nt * 128 + w2c * 64 + (ks >> 1) * 32 + (g & 1) * 16 + (g >> 1) * 8;
    *(u32x4*)(out + (size_t)(t_out + ti * 16) * D + e0) = v;
  }
  { const u64 te = __builtin_amdgcn_s_memrealtime() + 0 * (tpb - tpa);
    for (int i = 0; i < 100000 && __builtin_amdgcn_s_memrealtime() < te; ++i) __builtin_amdgcn_s_sleep(1); }
}

#if defined(__HIP_DEVICE_COMPILE__)
#pragma clang attribute pop
#endif
#define WS_OFF 0
extern "C" void kernel_launch(void* const* d_in, const int* in_sizes, int n_in, void* d_out, int out_size,
                              void* d_ws, size_t ws_size, hipStream_t stream) {
  k_fused<<<256, 512, 0, stream>>>((const u16*)d_in[0], (const u16*)d_in[1], (const u16*)d_in[2], (const u16*)d_in[3],
                                   (const u16*)d_in[4], (const u16*)d_in[6], (const u16*)d_in[7], (const u16*)d_in[8],
                                   (char*)d_ws + WS_OFF, (u16*)d_out);
}
```

```cpp
#include <hip/hip_runtime.h>
#include <hip/hip_bf16.h>
#include <stdint.h>
#include <utility>

#if defined(__HIP_DEVICE_COMPILE__)
#pragma clang attribute push(__attribute__((target("no-packed-fp32-ops"))), apply_to = function)
#endif
typedef unsigned short u16;
typedef unsigned long long u64;
typedef __attribute__((ext_vector_type(8))) short bf16x8;
typedef __attribute__((ext_vector_type(4))) short s16x4;
typedef __attribute__((ext_vector_type(4))) float f32x4;
typedef __attribute__((ext_vector_type(2))) float f32x2;
typedef __attribute__((ext_vector_type(2))) __bf16 bf2;
typedef __attribute__((ext_vector_type(4))) unsigned u32x4;
typedef __attribute__((ext_vector_type(2))) unsigned u32x2;
typedef __amdgpu_buffer_rsrc_t rsrc_t;
#define LDS_T(T) __attribute__((address_space(3))) T
#define SB() __builtin_amdgcn_sched_barrier(0)
#define DI __device__ __forceinline__
#define MFMA_IP(c, a, b) asm volatile("v_mfma_f32_16x16x32_bf16 %0, %1, %2, %0" : "+v"(c) : "v"(a), "v"(b))
#define MFMA_DRAIN() asm volatile("s_nop 7\n\ts_nop 3" ::: "memory")

constexpr int S = 2048, D = 512, F = 1792;
constexpr unsigned WS_AB = 0;
constexpr unsigned WS_H = WS_AB + 4096 * 512 * 2;
constexpr unsigned WS_P = WS_H + 28 * 2048 * 128;
constexpr unsigned WS_FLAG = WS_P + 64 * 16 * 8192;
constexpr unsigned FLAG_A = WS_FLAG, FLAG_B = WS_FLAG + 256 * 128, FLAG_E = WS_FLAG + 512 * 128;
constexpr unsigned WS_BYTES = FLAG_E + 128;
constexpr int CODE_BYTES = 26624;
constexpr int SLOT = 36864;
constexpr int L_CS1 = 4 * SLOT;
constexpr int L_CS2 = L_CS1 + 4096;
constexpr int L_MF = L_CS2 + 2048;
constexpr int L_VQ = L_MF + 2048;
constexpr int L_ZERO = L_VQ + 512;
constexpr int L_DUMMY = L_ZERO + 256;
constexpr int L_POLL = L_DUMMY + 256;
constexpr int LDS_BYTES = L_POLL + 5120;

template <int N> DI void wait_vm() { asm volatile("s_waitcnt vmcnt(%0)" ::"n"(N) : "memory"); }
DI void wait_lgkm0() { asm volatile("s_waitcnt lgkmcnt(0)" ::: "memory"); }
DI void bar() {
  asm volatile("" ::: "memory");
  __builtin_amdgcn_s_barrier();
  asm volatile("" ::: "memory");
}
DI unsigned pack2(float a, float b) {
  f32x2 p = {a, b};
  return __builtin_bit_cast(unsigned, __builtin_convertvector(p, bf2));
}
DI float bflo(unsigned u) { return __builtin_bit_cast(float, u << 16); }
DI float bfhi(unsigned u) { return __builtin_bit_cast(float, u & 0xffff0000u); }
DI rsrc_t mk_rsrc(const void* p, unsigned bytes) { return __builtin_amdgcn_make_buffer_rsrc((void*)p, 0, bytes, 0x00020000); }
template <int OFF> DI void dma16(rsrc_t r, unsigned lds, unsigned voff, unsigned soff) {
  asm volatile("s_add_u32 m0, %0, %4\n\ts_nop 0\n\tbuffer_load_dwordx4 %1, %2, %3 offen lds" ::"s"(lds), "v"(voff), "s"(r), "s"(soff), "n"(OFF) : "memory", "scc");
}
template <int OFF> DI void dma16c(rsrc_t r, unsigned lds, unsigned voff, unsigned soff) {
  asm volatile("s_add_u32 m0, %0, %4\n\ts_nop 0\n\tbuffer_load_dwordx4 %1, %2, %3 offen sc1 lds" ::"s"(lds), "v"(voff), "s"(r), "s"(soff), "n"(OFF) : "memory", "scc");
}
template <int OFF> DI void dma16_lo32(rsrc_t r, unsigned lds, unsigned voff, unsigned soff) {
  asm volatile("s_add_u32 m0, %0, %4\n\ts_mov_b32 exec_hi, 0\n\tbuffer_load_dwordx4 %1, %2, %3 offen lds\n\ts_mov_b32 exec_hi, -1"
               ::"s"(lds), "v"(voff), "s"(r), "s"(soff), "n"(OFF) : "memory", "scc");
}
DI void touch64(rsrc_t r, unsigned voff, unsigned soff) {
  asm volatile("s_mov_b32 m0, %3\n\ts_nop 0\n\tbuffer_load_dword %0, %1, %2 offen lds" ::"v"(voff), "s"(r), "s"(soff), "n"(L_DUMMY) : "memory");
}
DI void touch32(rsrc_t r, unsigned voff, unsigned soff) {
  asm volatile("s_mov_b32 m0, %3\n\ts_mov_b32 exec_hi, 0\n\tbuffer_load_dword %0, %1, %2 offen lds\n\ts_mov_b32 exec_hi, -1"
               ::"v"(voff), "s"(r), "s"(soff), "n"(L_DUMMY) : "memory");
}
DI void touch8(rsrc_t r, unsigned voff, unsigned soff) {
  asm volatile("s_mov_b32 m0, %3\n\ts_mov_b64 exec, 0xff\n\tbuffer_load_dword %0, %1, %2 offen lds\n\ts_mov_b64 exec, -1"
               ::"v"(voff), "s"(r), "s"(soff), "n"(L_DUMMY) : "memory");
}
DI void st16c(u32x4 v, rsrc_t r, unsigned voff, unsigned soff) {
  asm volatile("buffer_store_dwordx4 %0, %1, %2, %3 offen sc1\n\ts_nop 1" ::"v"(v), "v"(voff), "s"(r), "s"(soff) : "memory");
}
DI unsigned opqs(unsigned v) { asm volatile("" : "+s"(v)); return v; }
DI unsigned opq(unsigned v) { asm volatile("" : "+v"(v)); return v; }
template <class T> DI T ldsr(unsigned a) { return *(const LDS_T(T)*)(size_t)a; }
template <class T> DI void ldsw(unsigned a, T v) { *(LDS_T(T)*)(size_t)a = v; }
DI bf16x8 rd128(unsigned a) { return ldsr<bf16x8>(a); }
DI s16x4 rdtr1(unsigned a) { return __builtin_amdgcn_ds_read_tr16_b64_v4i16((LDS_T(s16x4)*)(size_t)a); }
DI u32x4 swap16(unsigned a0, unsigned a1, unsigned b0, unsigned b1) {
  auto s0 = __builtin_amdgcn_permlane16_swap(a0, b0, false, false);
  auto s1 = __builtin_amdgcn_permlane16_swap(a1, b1, false, false);
  return u32x4{s0[0], s1[0], s0[1], s1[1]};
}
DI unsigned umx(unsigned a, unsigned b) { return a > b ? a : b; }
DI bool all64(bool p) { return __builtin_amdgcn_ballot_w64(!p) == 0; }
DI u64 ld_flag(const char* p) { return __hip_atomic_load((const u64*)p, __ATOMIC_RELAXED, __HIP_MEMORY_SCOPE_AGENT); }
DI void st_flag(char* p, u64 v) { __hip_atomic_store((u64*)p, v, __ATOMIC_RELAXED, __HIP_MEMORY_SCOPE_AGENT); }
DI void spin_flags(const char* p, u64 tag) {
#pragma unroll 1
  for (unsigned spin = 0; spin < (1u << 20); ++spin) {
    if (all64(ld_flag(p) == tag)) break;
    __builtin_amdgcn_s_sleep(1);
  }
}

#define STAMP(k, i)
#define STAMPF(i)

template <int N> struct IC { static constexpr int value = N; };
template <class Fn, int... Is> DI void unroll_impl(Fn&& f, std::integer_sequence<int, Is...>) { (f(IC<Is>{}), ...); }
template <int N, class Fn> DI void unroll(Fn&& f) { unroll_impl(f, std::make_integer_sequence<int, N>{}); }
#define CV(x) decltype(x)::value

constexpr int st_ops(int stage) { return stage < 8 ? 5 : 4; }
#define X0H 3
#define PF1M 0x55
constexpr int PF1(int p) { return p >= 0 && p < 7 ? (PF1M >> p) & 1 : 0; }
constexpr int PFI(int p) { return __builtin_popcount(PF1M & ((1 << p) - 1)); }
#define PREFILL 0
constexpr int PRE0 = PREFILL == 0 ? 3 : 0;
constexpr int allowM(int p) { return p == 0 ? 5 + PRE0 : PF1(p - 2) + st_ops(p + 2) + PF1(p - 1); }

__global__ __launch_bounds__(512, 2) void k_fused(const u16* __restrict__ x, const u16* __restrict__ cre,
                                                  const u16* __restrict__ cim, const u16* __restrict__ wre,
                                                  const u16* __restrict__ wim, const u16* __restrict__ wg,
                                                  const u16* __restrict__ wu, const u16* __restrict__ wd,
                                                  char* __restrict__ ws, u16* __restrict__ out) {
  __shared__ __attribute__((aligned(1024))) char shm[LDS_BYTES];
  asm volatile("" ::"v"((LDS_T(char)*)shm));
  int lane = threadIdx.x & 63;
  const int wid = __builtin_amdgcn_readfirstlane(threadIdx.x >> 6);
  const int id = blockIdx.x, xcd = id & 7, idx = id >> 3;
  const int c = xcd >> 1, cgl = idx & 3;
  const int rb = (xcd & 1) * 8 + (idx >> 2);
  const bool isQ = cgl == 3;
  const int unit1 = 7 * c + 4 + cgl, unit2 = 7 * c + cgl;
  const int rot = ((idx >> 2) + 2 * cgl) & 7;
  int r = lane & 15, g = lane >> 4;
#define RELANE() do { lane = opq(__builtin_amdgcn_mbcnt_hi(~0u, __builtin_amdgcn_mbcnt_lo(~0u, 0u))); r = lane & 15; g = lane >> 4; } while (0)
#define sw ((r >> 1) & 7)
  const int wr = wid & 3, gq = wid >> 2;
  if (wid == 7) {
    const u64 pc = __builtin_amdgcn_s_getpc() & ~(u64)127;
    touch64(mk_rsrc((const void*)pc, CODE_BYTES), lane * 512, cgl * 128);
  }
  STAMP(0, 0);
#define FS(k)
#define FDUMP()

  const rsrc_t rx = mk_rsrc(x + rb * 128 * D, 128 * D * 2);
  const rsrc_t rre = mk_rsrc(wre + (c * 4 + cgl) * 32 * D, 32 * D * 2);
  const u16* wgu = gq ? wu : wg;
  const rsrc_t r1 = isQ ? mk_rsrc(wim + c * 128 * D, 128 * D * 2) : mk_rsrc(wgu + unit1 * 64, D * F * 2 - unit1 * 128);
  const rsrc_t r2 = mk_rsrc(wgu + unit2 * 64, D * F * 2 - unit2 * 128);
  const rsrc_t rc = mk_rsrc(((c & 2) ? cim : cre) + rb * 128 * S + (c & 1) * 1024, 128 * S * 2 - (c & 1) * 2048);
  const rsrc_t rws = mk_rsrc(ws, WS_BYTES);
  unsigned vrow[2], vw[2], v1[2];
#pragma unroll
  for (int i = 0; i < 2; ++i) {
    const int row = wid * 16 + i * 8 + (lane >> 3);
    vrow[i] = row * 1024 + (((lane & 7) ^ ((row >> 1) & 7)) << 4);
    const int kr = wr * 16 + i * 8 + (lane >> 3);
    const int f = ((kr >> 1) & 1) | (((kr >> 3) & 1) << 1);
    vw[i] = kr * (F * 2) + (((lane & 7) ^ (f << 1)) << 4);
    v1[i] = isQ ? vrow[i] : vw[i];
  }
  const unsigned vd32 = (wid * 4 + (lane >> 3)) * 1024 + (((lane & 7) ^ (((wid * 4 + (lane >> 3)) >> 1) & 7)) << 4);
  const unsigned step1 = isQ ? 128 : 64 * F * 2;
  const unsigned lw2 = wid * 2048, lw5 = wid * 512;
  auto issueA = [&](auto sc, auto qc) {
    constexpr int stage = CV(sc), q = CV(qc), sl = (stage & 3) * SLOT;
    if constexpr (stage < 8) {
      const int kp = (stage + rot) & 7;
      if constexpr (q < 2) dma16<sl + q * 1024>(rx, lw2, vrow[q], kp * 128);
      else if constexpr (q < 4) dma16<sl + 16384 + (q - 2) * 1024>(r1, lw2, v1[q - 2], kp * step1);
      else dma16_lo32<sl + 32768>(rre, lw5, vd32, kp * 128);
    } else if constexpr (q < 4) {
      const int kp = ((stage - 8) * 2 + (q >> 1) + rot) & 7;
      dma16<sl + (q >> 1) * 16384 + (q & 1) * 1024>(r2, lw2, vw[q & 1], kp * (64 * F * 2));
    }
  };
  unroll<5>([&](auto q) { issueA(IC<0>{}, q); });
  unroll<5>([&](auto q) { issueA(IC<1>{}, q); });
#pragma unroll
  for (int i = 0; i < 3; ++i)
    st16c(u32x4{~0u, ~0u, ~0u, ~0u}, rws, lane * 16, WS_P + ((rb * 4 + cgl) * 16 + c * 4 + ((c + 1 + i) & 3)) * 8192 + wid * 1024);
  u64 tag = *(const __attribute__((address_space(4))) u64*)(ws + FLAG_E) + 1;
  asm volatile("" : "+s"(tag));
  ldsw<u32x4>(L_ZERO, u32x4{0u, 0u, 0u, 0u});
  const unsigned vpf1 = ((lane >> 2) & 1) * (S * 2) + (lane & 3) * 128;

  const int jl = wr >> 1, bl = wr & 1;
  const unsigned xa = r * 128 + (((4 * jl + g) ^ sw) << 4);
  unsigned wa;
  {
    const int q = (lane & 15) >> 2, p = lane & 3;
    const int f = (q >> 1) | ((g & 1) << 1);
    wa = (8 * g + q) * 128 + (((2 * gq + bl) ^ f) << 5) + p * 8 + jl * 4096;
  }
  bf16x8 xf[8][2][2];
  s16x4 wh[4][2][2];
  bf16x8 df[2][2];
#define WF(f, j) __builtin_shufflevector(wh[f][j][0], wh[f][j][1], 0, 1, 2, 3, 4, 5, 6, 7)
  f32x4 acc[4][2], a0[2], ssum, ssq, cs0, cs1, csd;
  bf16x8 ones = {0x3f80, 0x3f80, 0x3f80, 0x3f80, 0x3f80, 0x3f80, 0x3f80, 0x3f80};
  const f32x4 z4 = {0.f, 0.f, 0.f, 0.f};
  unsigned xA[2], dA[2], wo[2];
#pragma unroll
  for (int b = 0; b < 2; ++b) { xA[b] = xa + (wr * 32 + (b ^ gq) * 16) * 128; dA[b] = xa + 32768 + (b ^ bl) * 2048; }
  auto base_w_tr = [&](unsigned st, int j) {
    const unsigned so = opqs(st);
    wo[0] = (wa ^ (j << 12)) + so;
    wo[1] = (wa ^ 32 ^ (j << 12)) + so;
  };
  auto read_w_tr1 = [&](int k, int j) {
    wh[k >> 1][j][k & 1] = rdtr1(wo[(k >> 1) & 1] + (k >> 2) * 8192 + (k & 1) * 512);
  };
  auto base_w_q = [&](unsigned st, int j) {
    const unsigned so = opqs(st) + gq * 8192 - 16384;
    wo[0] = (dA[0] ^ (j << 6)) + so;
    wo[1] = (dA[1] ^ (j << 6)) + so;
  };
  auto read_w_q1 = [&](int f, int j) {
    const bf16x8 v = rd128(wo[f & 1] + (f >> 1) * 4096);
    wh[f][j][0] = __builtin_shufflevector(v, v, 0, 1, 2, 3);
    wh[f][j][1] = __builtin_shufflevector(v, v, 4, 5, 6, 7);
  };
  unsigned xso;
  auto rd1 = [&](auto qc, int p, int j, int k) {
    constexpr bool Q = CV(qc);
    if (k == 0) { if (Q) base_w_q((p & 3) * SLOT, j); else base_w_tr((p & 3) * SLOT + 16384, j); xso = opqs((p & 3) * SLOT); }
    if (k == 2 || k == 3) xf[p][k - 2][j] = rd128((xA[k - 2] ^ (j << 6)) + xso);
    else if (k >= 10) df[k - 10][j] = rd128((dA[k - 10] ^ (j << 6)) + xso);
    else {
      const int kk = k < 2 ? k : k - 2;
      if (!Q) read_w_tr1(kk, j);
      else if ((kk & 1) == 0) read_w_q1(kk >> 1, j);
    }
  };
  auto period_op = [&](auto pc, auto kc) {
    constexpr int p = CV(pc), k = CV(kc);
    if constexpr (k < st_ops(p + 3)) issueA(IC<p + 3>{}, kc);
    else if constexpr (k == st_ops(p + 3) && PF1(p))
      touch8(rc, vpf1, (cgl * 32 + wid * 4 + (PFI(p) & 1) * 2) * (S * 2) + (PFI(p) >> 1) * 512);
  };
  auto pass1 = [&](auto qc) {
#pragma unroll
    for (int a = 0; a < 4; ++a) { acc[a][0] = z4; acc[a][1] = z4; }
    a0[0] = z4; a0[1] = z4; ssum = z4; ssq = z4; cs0 = z4; cs1 = z4; csd = z4;
    asm volatile("" : "+v"(acc[0][0]), "+v"(acc[0][1]), "+v"(acc[1][0]), "+v"(acc[1][1]), "+v"(acc[2][0]), "+v"(acc[2][1]), "+v"(acc[3][0]), "+v"(acc[3][1]), "+v"(a0[0]), "+v"(a0[1]));
    asm volatile("" : "+v"(ssum), "+v"(ssq), "+v"(cs0), "+v"(cs1), "+v"(csd), "+v"(ones));
    wait_vm<5 + PRE0>();
    bar();
    STAMP(0, 1);
#pragma unroll
    for (int k = 0; k < 12; ++k) rd1(qc, 0, 0, k);
    SB();
    unroll<16>([&](auto hc) {
      constexpr int h = CV(hc), p = h >> 1, j = h & 1;
      if constexpr (j == 1) { wait_vm<allowM(p)>(); bar(); }
      SB();
      unroll<14 - j>([&](auto nc) {
        constexpr int n = CV(nc);
        if constexpr (n < 8) MFMA_IP(acc[n >> 1][n & 1], WF(n >> 1, j), xf[p][n & 1][j]);
        else if constexpr (n == 8) MFMA_IP(ssum, ones, xf[p][0][j]);
        else if constexpr (n == 9) MFMA_IP(ssq, xf[p][0][j], xf[p][0][j]);
        else if constexpr (n == 10) { if constexpr (j == 0) MFMA_IP(cs0, ones, WF(0, 0)); else MFMA_IP(cs1, ones, WF(2, 1)); }
        else if constexpr (n < 13) MFMA_IP(a0[n - 11], df[n - 11][j], xf[p][0][j]);
        else MFMA_IP(csd, ones, df[0][0]);
        SB();
        constexpr int pn = (h + 1) >> 1, jn = (h + 1) & 1;
        if constexpr (h < 15) { if constexpr (n < 12) rd1(qc, pn, jn, n); }
        else { if constexpr (n == 0) base_w_tr(0, 0); if constexpr (n < 8) read_w_tr1(n, 0); }
        if constexpr (h == 0) { if constexpr (n % 3 == 1) issueA(IC<2>{}, IC<n / 3>{}); }
        else if constexpr (h == 15) { if constexpr (n % 3 == 2) period_op(IC<7>{}, IC<n / 3>{}); }
        else if constexpr (n == 2 || n == 6 || n == 10) {
          if constexpr (j == 1) period_op(IC<p>{}, IC<(n - 2) / 4>{});
          else period_op(IC<p - 1>{}, IC<3 + (n - 2) / 4>{});
        }
        SB();
      });
    });
  };
  if (isQ) pass1(IC<1>{}); else pass1(IC<0>{});
  FS(0);

  auto put_cs = [&](float v, unsigned addr) {
    const unsigned d1 = pack2(v, 0.f);
    ldsw<u32x2>(addr, u32x2{pack2(v, v - bflo(d1)), d1});
  };
  RELANE();
  {
    const unsigned row = wr * 32 + gq * 16 + r;
    asm volatile("s_nop 7\n\ts_nop 3" : "+v"(cs0), "+v"(cs1), "+v"(csd), "+v"(ssum), "+v"(ssq));
    ldsw<f32x4>(gq * SLOT + 32768 + wr * 1024 + lane * 16, ssq);
    const float c01 = g == 0 ? cs0[0] : cs1[0];
    put_cs(g < 2 ? c01 : csd[0], L_CS1 + gq * 2048 + bl * 256 + jl * 8 + g * 512 + r * 16);
    const float nm = ssum[0] * (-1.0f / D);
    const unsigned d0 = pack2(nm, nm), d1 = pack2(nm - bflo(d0), 0.f);
    ldsw<u32x4>(L_MF + row * 16, u32x4{d0, d1, d0, d1});
    ldsw<float>(L_VQ + row * 4, fmaf(-nm, nm, 1e-5f));
  }
  wait_lgkm0();
  FS(1);
  wait_vm<PF1(6) + 4>();
  bar();
  FS(2);
  STAMP(0, 2);

  float var[2];
  bf16x8 mf[2], cf[4], cd[2];
  auto corr_rd = [&](f32x4 (&ac_)[4][2], unsigned csbase) {
    asm volatile("" : "+v"(ac_[0][0]), "+v"(ac_[0][1]), "+v"(ac_[1][0]), "+v"(ac_[1][1]), "+v"(ac_[2][0]), "+v"(ac_[2][1]), "+v"(ac_[3][0]), "+v"(ac_[3][1]));
    const unsigned r16 = r * 16;
#pragma unroll
    for (int b = 0; b < 2; ++b) mf[b] = rd128(g == 0 ? L_MF + (wr * 32 + (b ^ gq) * 16) * 16 + r16 : L_ZERO);
    const unsigned ca0 = csbase + bl * 256 + r16, ca1 = csbase + (bl ^ 1) * 256 + r16;
    cf[0] = rd128(ca0); cf[1] = rd128(ca1); cf[2] = rd128(ca0 + 512); cf[3] = rd128(ca1 + 512);
    return u32x2{ca0, ca1};
  };
  auto corr_mm = [&](f32x4 (&ac_)[4][2]) {
    SB();
#pragma unroll
    for (int b = 0; b < 2; ++b)
#pragma unroll
      for (int f = 0; f < 4; ++f) MFMA_IP(ac_[f][b], cf[f], mf[b]);
    MFMA_DRAIN();
    SB();
  };
  float K1, K2;
  auto ffn_k = [&](int b) {
    const float rs = __builtin_amdgcn_rsqf(var[b]);
    K1 = -2.302208198f * rs; K2 = -0.1029432397f * rs * rs * rs;
  };
  auto ffn_pair = [&](const f32x4 (&ac_)[4][2], int b, int k) {
    float h[2];
#pragma unroll
    for (int q = 0; q < 2; ++q) {
      const float a = ac_[k >> 1][b][(k & 1) * 2 + q];
      const float e = __builtin_amdgcn_exp2f(a * fmaf(a * a, K2, K1));
      h[q] = a * ac_[2 + (k >> 1)][b][(k & 1) * 2 + q] * __builtin_amdgcn_rcpf(fmaf(e, var[b], var[b]));
    }
    return pack2(h[0], h[1]);
  };
  auto ffn_calc = [&](const f32x4 (&ac_)[4][2], int b) {
    ffn_k(b);
    const unsigned o0 = ffn_pair(ac_, b, 0), o1 = ffn_pair(ac_, b, 1), o2 = ffn_pair(ac_, b, 2), o3 = ffn_pair(ac_, b, 3);
    return swap16(o0, o1, o2, o3);
  };
#define VST (r * 64 + (((g & 1) ^ bl) * 2 + (g >> 1)) * 16)
  auto ffn_store = [&](u32x4 v, int unit, int b) {
    st16c(v, rws, VST, WS_H + ((unit * 2 + gq) * S + rb * 128 + wr * 32 + (b ^ gq) * 16) * 64);
  };
  auto dft_store = [&](f32x4 v0, f32x4 v1_, float rs, int grow, int eblk) {
    const u32x4 v = swap16(pack2(v0[0] * rs, v0[1] * rs), pack2(v0[2] * rs, v0[3] * rs), pack2(v1_[0] * rs, v1_[1] * rs), pack2(v1_[2] * rs, v1_[3] * rs));
    st16c(v, rws, VST, WS_AB + (eblk * 4096 + grow) * 64);
  };
  {
    const u32x2 ca = corr_rd(acc, L_CS1 + gq * 2048);
    cd[0] = rd128(ca[0] + 1024); cd[1] = rd128(ca[1] + 1024);
    const unsigned dg = (r >> 2) * 256 + r * 16 + (r & 3) * 4;
#pragma unroll
    for (int b = 0; b < 2; ++b)
      var[b] = fmaxf(fmaf(ldsr<float>((b ^ gq) * SLOT + 32768 + wr * 1024 + dg), 1.0f / D, ldsr<float>(L_VQ + (wr * 32 + (b ^ gq) * 16 + r) * 4)), 1e-5f);
    SB();
    unroll<4>([&](auto q) { issueA(IC<11>{}, q); });
    SB();
    MFMA_IP(a0[0], cd[0], mf[0]); MFMA_IP(a0[1], cd[1], mf[0]);
    corr_mm(acc);
  }
  FS(3);
  dft_store(a0[0], a0[1], __builtin_amdgcn_rsqf(var[0]), rb * 128 + wr * 32 + gq * 16, c * 4 + cgl);
  if (isQ) {
#pragma unroll
    for (int b = 0; b < 2; ++b) {
      const float rs = -__builtin_amdgcn_rsqf(var[b]);
#pragma unroll
      for (int h = 0; h < 2; ++h)
        dft_store(acc[2 * h][b], acc[2 * h + 1][b], rs, S + rb * 128 + wr * 32 + (b ^ gq) * 16, c * 4 + gq * 2 + h);
    }
  }
  FS(4);
  const int ks = c, mt = rb, nt = cgl, tile = mt * 4 + nt;
  f32x4 ac2[4][2];
  auto half2 = [&](auto hc) {
    constexpr int h = CV(hc), p = h >> 1, j = h & 1;
    unroll<9>([&](auto nc) {
      constexpr int n = CV(nc);
      if constexpr (n < 8) MFMA_IP(ac2[n >> 1][n & 1], WF(n >> 1, j), xf[p][n & 1][j]);
      else if constexpr (j == 0) MFMA_IP(cs0, ones, WF(0, 0));
      else MFMA_IP(cs1, ones, WF(2, 1));
      SB();
      constexpr int pn = (h + 1) >> 1, jn = (h + 1) & 1;
      if constexpr (h < 15) {
        if constexpr (n == 0) base_w_tr(((8 + (pn >> 1)) & 3) * SLOT + (pn & 1) * 16384, jn);
        if constexpr (n < 8) read_w_tr1(n, jn);
      }
      SB();
    });
  };
  unsigned va[4], vb[4];
  auto init_vab = [&](auto hc) {
    const int r0 = wr * 32 + (lane >> 3), k0 = wr * 16 + (lane >> 4);
#pragma unroll
    for (int i = 0; i < 4; ++i) {
      const int row = r0 + i * 8, kr = k0 + i * 4;
      const int ca = (lane & 7) ^ ((row >> 1) & 7), cb = (lane & 15) ^ (((kr & 3) | (((kr >> 3) & 1) << 2)) << 1);
      va[i] = CV(hc) ? (ca >> 2) * (S * 64) + row * 64 + (ca & 3) * 16 : row * (S * 2) + ca * 16;
      vb[i] = CV(hc) ? kr * 1024 + cb * 16 : (cb >> 2) * (4096 * 64) + kr * 64 + (cb & 3) * 16;
    }
  };
  auto issueC1 = [&](int step, auto qc) {
    constexpr int q = CV(qc);
    const unsigned dst = (step & 3) * SLOT + wr * 4096;
    if constexpr (q < 4) dma16<q * 1024>(rc, dst, va[q], step * 128);
    else dma16c<16384 + (q - 4) * 1024>(rws, dst, vb[q - 4], WS_AB + (nt * 4 * 4096 + ks * 1024 + step * 64) * 64);
  };
  auto issueC = [&](int step) { unroll<8>([&](auto q) { issueC1(step, q); }); };
  auto touchC = [&](int quarter) {
    touch32(rc, (lane >> 2) * (S * 2) + (lane & 3) * 128, (nt * 32 + wr * 8) * (S * 2) + quarter * 512);
  };

  if (gq == 0) {
    u32x4 hv;
    if (!isQ) hv = ffn_calc(acc, 0);
    FS(5);
    wait_vm<0>();
    FS(6);
    bar();
    FS(7);
    if (wid == 0 && lane == 0) st_flag(ws + FLAG_A + id * 128, tag);
    touchC(2);
    if (!isQ) ffn_store(hv, unit1, 0);
    if (!isQ) ffn_store(ffn_calc(acc, 1), unit1, 1);
  }
  STAMP(0, 3);
  FS(8);
#pragma unroll
  for (int a = 0; a < 4; ++a) { ac2[a][0] = z4; ac2[a][1] = z4; }
  cs0 = z4; cs1 = z4;
  asm volatile("s_nop 1" : "+v"(ac2[0][0]), "+v"(ac2[0][1]), "+v"(ac2[1][0]), "+v"(ac2[1][1]), "+v"(ac2[2][0]), "+v"(ac2[2][1]), "+v"(ac2[3][0]), "+v"(ac2[3][1]), "+v"(cs0), "+v"(cs1));
  SB();
  unroll<X0H>([&](auto hc) { half2(hc); });
  FS(9);
  if (gq == 1) { wait_vm<0>(); FS(10); bar(); }
  FS(11);
  unroll<8 - X0H>([&](auto hc) { half2(IC<X0H + CV(hc)>{}); });
  FS(12);
  if (gq == 1) {
    RELANE();
    if (!isQ) { ffn_store(ffn_calc(acc, 0), unit1, 0); ffn_store(ffn_calc(acc, 1), unit1, 1); }
    init_vab(IC<0>{});
    FS(15);
    bar();
    FS(16);
    dma16c<L_POLL>(rws, wr * 1024, (lane & 31) * 1024, FLAG_A + (2 * cgl + (c & 1)) * 128);
    unroll<4>([&](auto q) { issueC1(0, q); });
    unroll<4>([&](auto q) { issueC1(1, q); });
    wait_vm<8>();
    FS(17);
    if (!all64(ldsr<u64>(L_POLL + wr * 1024 + lane * 16) == tag)) spin_flags(ws + FLAG_A + ((2 * cgl + (c & 1)) + 8 * (lane & 31)) * 128, tag);
    FS(13);
    unroll<4>([&](auto q) { issueC1(0, IC<4 + CV(q)>{}); });
    unroll<4>([&](auto q) { issueC1(1, IC<4 + CV(q)>{}); });
    FS(18);
    bar();
  }
  FS(19);
  SB();
  half2(IC<8>{});
  FS(20);
  if (gq == 0) { bar(); RELANE(); touchC(3); }
  FS(21);
  unroll<7>([&](auto hc) { half2(IC<9 + CV(hc)>{}); });
  FS(24);
  RELANE();
  asm volatile("s_nop 7\n\ts_nop 3" : "+v"(cs0), "+v"(cs1));
  put_cs((g & 1) ? cs1[0] : cs0[0], L_CS2 + gq * 1024 + bl * 256 + jl * 8 + (g & 1) * 512 + r * 16);
  wait_lgkm0();
  FS(25);

  if (gq == 1) {
    init_vab(IC<0>{});
    wait_vm<0>();
    FS(26);
    bar();
    FS(27);
    corr_rd(ac2, L_CS2 + 1024);
    unroll<4>([&](auto q) { issueC1(2, q); });
    FS(28);
    bar();
    FS(29);
    unroll<4>([&](auto q) { issueC1(2, IC<4 + CV(q)>{}); });
    corr_mm(ac2);
    unsigned o[4];
    auto slice = [&](auto kc) {
      constexpr int k = CV(kc), b = k / 6, t = k % 6;
      if constexpr (b < 2) {
        if constexpr (t == 0) ffn_k(b);
        else if constexpr (t < 5) o[t - 1] = opq(ffn_pair(ac2, b, t - 1));
        else ffn_store(swap16(o[0], o[1], o[2], o[3]), unit2, b);
      }
      SB();
    };
    unroll<8>([&](auto q) { issueC1(3, q); SB(); if constexpr (CV(q) & 1) slice(IC<CV(q) / 2>{}); });
    wait_vm<8>();
    FS(30);
    bar();
    FS(31);
    unroll<8>([&](auto q) { issueC1(4, q); SB(); if constexpr (CV(q) & 1) slice(IC<4 + CV(q) / 2>{}); });
    wait_vm<9>();
    FS(32);
    bar();
    FS(33);
    unroll<8>([&](auto q) { issueC1(5, q); SB(); if constexpr (CV(q) & 1) slice(IC<8 + CV(q) / 2>{}); });
    wait_vm<9>();
    FS(34);
    bar();
    issueC(6);
    wait_vm<8>();
    bar();
    FS(35);
    wait_vm<0>();
    FDUMP();
    issueC(7);
    const unsigned fbo = FLAG_B + ((lane & 4) ? (id & ~6) + 2 * (lane & 3) : xcd + 8 * ((idx & ~3) + (lane & 3))) * 128;
#pragma unroll 1
    for (int i = 5; i < 13; ++i) {
      wait_vm<8>();
      if (i == 12) {
        if (wr == 0 && !all64(ldsr<u64>(L_POLL + 4096 + lane * 16) == tag)) spin_flags(ws + fbo, tag);
        if (wr == 1 && id == 0) {
#pragma unroll 1
          for (int q = 0; q < 4; ++q)
            if (!all64(ldsr<u64>(L_CS1 + q * 1024 + lane * 16) == tag)) spin_flags(ws + FLAG_A + (q * 64 + lane) * 128, tag);
          if (lane == 0) st_flag(ws + FLAG_E, tag);
        }
      }
      bar();
      if (i == 5 && wr == 2 && lane == 0) st_flag(ws + FLAG_B + id * 128, tag);
      issueC(i + 3);
      if (i == 10) {
        if (wr == 0) dma16c<L_POLL + 4096>(rws, 0, fbo, 0);
        if (wr == 1 && id == 0) unroll<4>([&](auto q) { dma16c<L_CS1 + CV(q) * 1024>(rws, 0, lane * 128, FLAG_A + CV(q) * 8192); });
      }
    }
    init_vab(IC<1>{});
    const rsrc_t rd = mk_rsrc(wd, F * D * 2);
#pragma unroll 1
    for (int i = 13; i < 20; ++i) {
      wait_vm<8>();
      bar();
      const unsigned dst = ((i + 3) & 3) * SLOT + wr * 4096;
      const unsigned so = WS_H + ((ks * 7 + i - 13) * 2 * S + mt * 128) * 64, sb = (ks * 7 + i - 13) * 65536 + nt * 256;
      unroll<4>([&](auto q) { dma16c<CV(q) * 1024>(rws, dst, va[CV(q)], so); });
      unroll<4>([&](auto q) { dma16<16384 + CV(q) * 1024>(rd, dst, vb[CV(q)], sb); });
    }
    wait_vm<8>();
    bar();
    wait_vm<0>();
    bar();
    return;
  }

  bar();
  FS(26);
  STAMP(0, 4);
  corr_rd(ac2, L_CS2);
  touch32(mk_rsrc(wd, F * D * 2), (lane < 28 ? lane : 27) * 128, (ks * 448 + idx * 14) * 1024 + wr * 3584);
  corr_mm(ac2);
  FS(27);
  ffn_store(ffn_calc(ac2, 0), unit2, 0);
  FS(28);
  ffn_store(ffn_calc(ac2, 1), unit2, 1);
  FS(31);
  STAMP(0, 5);

  const int w2r = wr >> 1, w2c = wr & 1;
  f32x4 bc[4][4];
#pragma unroll
  for (int a = 0; a < 4; ++a)
#pragma unroll
    for (int b = 0; b < 4; ++b) bc[a][b] = z4;
  unsigned ac[2], bd[4];
#pragma unroll
  for (int j = 0; j < 2; ++j) ac[j] = (w2r * 64 + r) * 128 + (((4 * j + g) ^ sw) << 4);
  {
    const int q = (lane & 15) >> 2, p = lane & 3;
    const int f = q | ((g & 1) << 2);
#pragma unroll
    for (int e = 0; e < 4; ++e) bd[e] = 16384 + (8 * g + q) * 256 + (((w2c * 4 + e) ^ f) << 5) + p * 8;
  }
  bf16x8 fa[2][4];
  s16x4 fb[2][4][2];
  int dlt = 2 * SLOT;
  auto sub = [&](auto mbc, auto spc, auto jc, auto rdc, auto tgc) {
    constexpr int mb = CV(mbc), nb = mb ^ 1, j = CV(jc);
    constexpr unsigned so = CV(spc) * SLOT;
    unroll<16>([&](auto nc) {
      constexpr int n = CV(nc), ei = n >> 2, ti = n & 3;
      MFMA_IP(bc[ei][ti], __builtin_shufflevector(fb[mb][ei][0], fb[mb][ei][1], 0, 1, 2, 3, 4, 5, 6, 7), fa[mb][ti]);
      SB();
      if constexpr (CV(rdc)) {
        if constexpr (n == 0) fa[nb][0] = rd128(ac[j] + so);
        if constexpr (n == 1 || n == 2) fb[nb][0][n - 1] = rdtr1(bd[0] + so + j * 8192 + (n - 1) * 1024);
        if constexpr (n == 3) fa[nb][1] = rd128(ac[j] + so + 2048);
        if constexpr (n == 5) fa[nb][2] = rd128(ac[j] + so + 4096);
        if constexpr (n == 7) fa[nb][3] = rd128(ac[j] + so + 6144);
        if constexpr (n >= 8 && n < 14) fb[nb][(n - 6) >> 1][n & 1] = rdtr1(bd[(n - 6) >> 1] + so + j * 8192 + (n & 1) * 1024);
      }
      if constexpr (CV(tgc)) {
        if constexpr (n == 4) { ac[0] = opq(ac[0] + dlt); }
        if constexpr (n == 6) { bd[0] = opq(bd[0] + dlt); }
        if constexpr (n == 14) { ac[1] = opq(ac[1] + dlt); bd[1] = opq(bd[1] + dlt); }
        if constexpr (n == 15) { bd[2] = opq(bd[2] + dlt); bd[3] = opq(bd[3] + dlt); dlt = -dlt; }
      }
      SB();
    });
  };
#pragma unroll
  for (int a = 0; a < 4; ++a) asm volatile("" : "+v"(bc[a][0]), "+v"(bc[a][1]), "+v"(bc[a][2]), "+v"(bc[a][3]));
  FS(32);
  bar();
  FS(33);
  FDUMP();
  STAMP(0, 6);
#pragma unroll
  for (int i = 0; i < 4; ++i) {
    fa[0][i] = rd128(ac[0] + i * 2048);
    fb[0][i][0] = rdtr1(bd[i]);
    fb[0][i][1] = rdtr1(bd[i] + 1024);
  }
  SB();
#pragma unroll 1
  for (int i = 0; i < 22; i += 2) {
    sub(IC<0>{}, IC<0>{}, IC<1>{}, IC<1>{}, IC<0>{});
    bar();
    sub(IC<1>{}, IC<1>{}, IC<0>{}, IC<1>{}, IC<0>{});
    sub(IC<0>{}, IC<1>{}, IC<1>{}, IC<1>{}, IC<1>{});
    wait_vm<0>();
    bar();
    sub(IC<1>{}, IC<0>{}, IC<0>{}, IC<1>{}, IC<0>{});
  }
  RELANE();
  const int t_out = mt * 128 + w2r * 64 + (ks & 1) * 32 + r;
  const int e_out = nt * 128 + w2c * 64 + (ks >> 1) * 32 + g * 4;
  u32x2 xr[2][2];
#pragma unroll
  for (int ei = 0; ei < 2; ++ei)
#pragma unroll
    for (int ti = 0; ti < 2; ++ti) xr[ei][ti] = *(const u32x2*)(x + (size_t)(t_out + ti * 16) * D + e_out + ei * 16);
  SB();
  sub(IC<0>{}, IC<0>{}, IC<1>{}, IC<1>{}, IC<0>{});
  sub(IC<1>{}, IC<0>{}, IC<0>{}, IC<0>{}, IC<0>{});
  MFMA_DRAIN();
  SB();
  STAMP(0, 9);

  f32x4 own[2][2];
#pragma unroll
  for (int q = 0; q < 4; ++q) {
    const int e2 = (q >> 1) * 2, t2 = (q & 1) * 2;
    if (q == ks) {
#pragma unroll
      for (int ei = 0; ei < 2; ++ei)
#pragma unroll
        for (int ti = 0; ti < 2; ++ti) own[ei][ti] = bc[e2 + ei][t2 + ti];
    } else {
#pragma unroll
      for (int ei = 0; ei < 2; ++ei) {
        const f32x4 a = bc[e2 + ei][t2], b = bc[e2 + ei][t2 + 1];
        st16c(u32x4{pack2(a[0], a[1]), pack2(a[2], a[3]), pack2(b[0], b[1]), pack2(b[2], b[3])}, rws, lane * 16,
              WS_P + (tile * 16 + q * 4 + ks) * 8192 + wr * 2048 + ei * 1024);
      }
    }
  }
  STAMP(0, 10);
  u32x4 pv[6];
#pragma unroll 1
  for (unsigned spin = 0; spin < (1u << 20); ++spin) {
    unsigned mx = 0;
#pragma unroll
    for (int i = 0; i < 6; ++i)
      pv[i] = __builtin_amdgcn_raw_buffer_load_b128(rws, lane * 16, WS_P + (tile * 16 + ks * 4 + ((ks + 1 + (i >> 1)) & 3)) * 8192 + wr * 2048 + (i & 1) * 1024, 16);
#pragma unroll
    for (int i = 0; i < 6; ++i) mx = umx(umx(mx, umx(pv[i][0], pv[i][1])), umx(pv[i][2], pv[i][3]));
    if (all64(mx != ~0u)) break;
    asm volatile("" ::: "memory");
  }
  STAMP(0, 11);
#pragma unroll
  for (int i = 0; i < 3; ++i)
#pragma unroll
    for (int ei = 0; ei < 2; ++ei)
#pragma unroll
      for (int ti = 0; ti < 2; ++ti) {
        own[ei][ti][0] += bflo(pv[2 * i + ei][2 * ti]);
        own[ei][ti][1] += bfhi(pv[2 * i + ei][2 * ti]);
        own[ei][ti][2] += bflo(pv[2 * i + ei][2 * ti + 1]);
        own[ei][ti][3] += bfhi(pv[2 * i + ei][2 * ti + 1]);
      }
#pragma unroll
  for (int ti = 0; ti < 2; ++ti) {
    unsigned o[2][2];
#pragma unroll
    for (int ei = 0; ei < 2; ++ei) {
      o[ei][0] = pack2(own[ei][ti][0] + bflo(xr[ei][ti][0]), own[ei][ti][1] + bfhi(xr[ei][ti][0]));
      o[ei][1] = pack2(own[ei][ti][2] + bflo(xr[ei][ti][1]), own[ei][ti][3] + bfhi(xr[ei][ti][1]));
    }
    const u32x4 v = swap16(o[0][0], o[0][1], o[1][0], o[1][1]);
    const int e0 = nt * 128 + w2c * 64 + (ks >> 1) * 32 + (g & 1) * 16 + (g >> 1) * 8;
    *(u32x4*)(out + (size_t)(t_out + ti * 16) * D + e0) = v;
  }
  STAMP(0, 12);
}

#if defined(__HIP_DEVICE_COMPILE__)
#pragma clang attribute pop
#endif
#define WS_OFF 0
extern "C" void kernel_launch(void* const* d_in, const int* in_sizes, int n_in, void* d_out, int out_size,
                              void* d_ws, size_t ws_size, hipStream_t stream) {
  k_fused<<<256, 512, 0, stream>>>((const u16*)d_in[0], (const u16*)d_in[1], (const u16*)d_in[2], (const u16*)d_in[3],
                                   (const u16*)d_in[4], (const u16*)d_in[6], (const u16*)d_in[7], (const u16*)d_in[8],
                                   (char*)d_ws + WS_OFF, (u16*)d_out);
}
```
